```python
import torch

def _softmax(x, axis=-1):
    return torch.softmax(x, dim=axis)
def _gelu(x):
    return torch.nn.functional.gelu(x, approximate="tanh")
def _silu(x):
    return torch.nn.functional.silu(x)
def _sigmoid(x):
    return torch.sigmoid(x)
def _layernorm(x, axis=-1, eps=1e-5):
    m = x.mean(dim=axis, keepdim=True)
    v = x.var(dim=axis, keepdim=True, unbiased=False)
    return (x - m) / torch.sqrt(v + eps)
def _scatter_add(tgt, idx, src, axis=0):
    return tgt.clone().index_add_(axis, idx, src)
def _segment_sum(src, seg, n):
    out = torch.zeros((n,) + src.shape[1:], dtype=src.dtype)
    return out.index_add_(0, seg, src)
def _pad(x, pw):
    flat = tuple(v for pair in reversed(pw) for v in pair)
    return torch.nn.functional.pad(x, flat)

kernel_name = 'standalone__glu_gated_bilinear__prompt'

def setup_inputs():

    _w = (lambda a: (lambda t: t.to(torch.bfloat16) if t.is_floating_point() else t)(torch.as_tensor(a)))
    B, S, D, F = 1, 2048, 512, 1792
    return {
        "x": _w((lambda *s: torch.randn(*s, dtype=torch.bfloat16) * 0.05)(B, S, D)),
        "w_gate": _w((lambda *s: torch.randn(*s, dtype=torch.bfloat16) * 0.05)(D, F) * (D ** -0.5)),
        "w_up": _w((lambda *s: torch.randn(*s, dtype=torch.bfloat16) * 0.05)(D, F) * (D ** -0.5)),
        "w_down": _w((lambda *s: torch.randn(*s, dtype=torch.bfloat16) * 0.05)(F, D) * (F ** -0.5)),
    }

def reference(x, w_gate, w_up, w_down):
    g = torch.einsum("bsd,df->bsf", x, w_gate)
    u = torch.einsum("bsd,df->bsf", x, w_up)
    h = g * u
    return torch.einsum("bsf,fd->bsd", h, w_down)
```

```cpp
#include <hip/hip_runtime.h>
#include <hip/hip_bf16.h>
typedef short bf16x8 __attribute__((ext_vector_type(8)));
typedef float f32x16 __attribute__((ext_vector_type(16)));
typedef unsigned short u16;
constexpr int S=2048, D=512, F=1792;

__device__ __forceinline__ unsigned short f2bf(float x){ __hip_bfloat16 b = __float2bfloat16(x); return *reinterpret_cast<unsigned short*>(&b); }

__global__ __launch_bounds__(256) void k1(const u16* x, const u16* wg, const u16* wu, u16* h){
  int wave = blockIdx.x*4 + (threadIdx.x>>6);
  int lane = threadIdx.x&63;
  int ft = wave % 112, st = wave / 112;
  int r = lane&31, hh = lane>>5;
  const u16* w = (r<16)? wg : wu;
  int f = ft*16 + (r&15);
  f32x16 acc = {};
  for(int k0=0;k0<D;k0+=16){
    bf16x8 a,b;
    for(int j=0;j<8;++j) a[j] = (short)w[(k0+8*hh+j)*F + f];
    b = *(const bf16x8*)(x + (st*32+r)*D + k0+8*hh);
    acc = __builtin_amdgcn_mfma_f32_32x32x16_bf16(a,b,acc,0,0,0);
  }
  for(int i=0;i<8;++i){
    int row = (i&3)+8*(i>>2)+4*hh;
    h[(st*32+r)*F + ft*16+row] = f2bf(acc[i]*acc[i+8]);
  }
}
__global__ __launch_bounds__(256) void k2(const u16* h, const u16* wd, u16* out){
  int wave = blockIdx.x*4 + (threadIdx.x>>6);
  int lane = threadIdx.x&63;
  int dt = wave % 16, st = wave / 16;
  int r = lane&31, hh = lane>>5;
  f32x16 acc = {};
  for(int k0=0;k0<F;k0+=16){
    bf16x8 a,b;
    for(int j=0;j<8;++j) a[j] = (short)wd[(k0+8*hh+j)*D + dt*32+r];
    b = *(const bf16x8*)(h + (st*32+r)*F + k0+8*hh);
    acc = __builtin_amdgcn_mfma_f32_32x32x16_bf16(a,b,acc,0,0,0);
  }
  for(int i=0;i<16;++i){
    int row = (i&3)+8*(i>>2)+4*hh;
    out[(st*32+r)*D + dt*32+row] = f2bf(acc[i]);
  }
}
extern "C" void kernel_launch(void* const* d_in, const int* in_sizes, int n_in,
                              void* d_out, int out_size, void* d_ws, size_t ws_size,
                              hipStream_t stream) {
  const u16* x=(const u16*)d_in[0]; const u16* wg=(const u16*)d_in[1]; const u16* wu=(const u16*)d_in[2]; const u16* wd=(const u16*)d_in[3];
  u16* h=(u16*)d_ws;
  k1<<<64*112/4,256,0,stream>>>(x,wg,wu,h);
  k2<<<64*16/4,256,0,stream>>>(h,wd,(u16*)d_out);
}
```

```cpp
#include <hip/hip_runtime.h>
#include <hip/hip_bf16.h>
typedef short bf16x8 __attribute__((ext_vector_type(8)));
typedef float f32x16 __attribute__((ext_vector_type(16)));
typedef unsigned short u16;
constexpr int S=2048, D=512, F=1792;

__device__ __forceinline__ unsigned short f2bf(float x){ __hip_bfloat16 b = __float2bfloat16(x); return *reinterpret_cast<unsigned short*>(&b); }

__global__ __launch_bounds__(256) void k1(const u16* x, const u16* wg, const u16* wu, u16* h){
  int wave = blockIdx.x*4 + (threadIdx.x>>6);
  int lane = threadIdx.x&63;
  int ft = wave % 112, st = wave / 112;
  int r = lane&31, hh = lane>>5;
  const u16* w = (r<16)? wg : wu;
  int f = ft*16 + (r&15);
  f32x16 acc = {};
  for(int k0=0;k0<D;k0+=16){
    bf16x8 a,b;
    for(int j=0;j<8;++j) a[j] = (short)w[(k0+8*hh+j)*F + f];
    b = *(const bf16x8*)(x + (st*32+r)*D + k0+8*hh);
    acc = __builtin_amdgcn_mfma_f32_32x32x16_bf16(a,b,acc,0,0,0);
  }
  for(int i=0;i<8;++i){
    int row = (i&3)+8*(i>>2)+4*hh;
    h[(st*32+r)*F + ft*16+row] = f2bf(acc[i]*acc[i+8]);
  }
}
__global__ __launch_bounds__(256) void k2(const u16* h, const u16* wd, u16* out){
  int wave = blockIdx.x*4 + (threadIdx.x>>6);
  int lane = threadIdx.x&63;
  int dt = wave % 16, st = wave / 16;
  int r = lane&31, hh = lane>>5;
  f32x16 acc = {};
  for(int k0=0;k0<F;k0+=16){
    bf16x8 a,b;
    for(int j=0;j<8;++j) a[j] = (short)wd[(k0+8*hh+j)*D + dt*32+r];
    b = *(const bf16x8*)(h + (st*32+r)*F + k0+8*hh);
    acc = __builtin_amdgcn_mfma_f32_32x32x16_bf16(a,b,acc,0,0,0);
  }
  for(int i=0;i<16;++i){
    int row = (i&3)+8*(i>>2)+4*hh;
    out[(st*32+r)*D + dt*32+row] = f2bf(acc[i]);
  }
}

typedef unsigned long long u64;
__device__ __forceinline__ u64 ct(){ u64 t; asm volatile("s_memtime %0\n\ts_waitcnt lgkmcnt(0)":"=s"(t)::"memory"); return t; }
__device__ __forceinline__ void spin_ticks(u64 n){ u64 t0=__builtin_amdgcn_s_memrealtime(); while(__builtin_amdgcn_s_memrealtime()-t0 < n) __builtin_amdgcn_s_sleep(1); }
__device__ __forceinline__ unsigned lat(const unsigned* p){ unsigned v; u64 a=ct(); asm volatile("global_load_dword %0, %1, off\n\ts_waitcnt vmcnt(0)":"=v"(v):"v"(p):"memory"); u64 b=ct(); asm volatile(""::"v"(v)); return (unsigned)(b-a); }

__global__ void probe(const unsigned* x, const unsigned* wd, const unsigned* ws){
  unsigned L0 = lat(wd); asm volatile(""::"s"(L0)); unsigned L = lat(wd+ 4096);
  spin_ticks((u64)L*10);
}
extern "C" void kernel_launch(void* const* d_in, const int* in_sizes, int n_in,
                              void* d_out, int out_size, void* d_ws, size_t ws_size,
                              hipStream_t stream) {
  const u16* x=(const u16*)d_in[0]; const u16* wg=(const u16*)d_in[1]; const u16* wu=(const u16*)d_in[2]; const u16* wd=(const u16*)d_in[3];
  u16* h=(u16*)d_ws;
  probe<<<1,64,0,stream>>>((const unsigned*)x,(const unsigned*)wd,(const unsigned*)d_ws + (64<<20)/4);
  k1<<<64*112/4,256,0,stream>>>(x,wg,wu,h);
  k2<<<64*16/4,256,0,stream>>>(h,wd,(u16*)d_out);
}
```

```cpp
#include <hip/hip_runtime.h>
#include <hip/hip_bf16.h>
typedef short bf16x8 __attribute__((ext_vector_type(8)));
typedef float f32x16 __attribute__((ext_vector_type(16)));
typedef unsigned short u16;
constexpr int S=2048, D=512, F=1792;

__device__ __forceinline__ unsigned short f2bf(float x){ __hip_bfloat16 b = __float2bfloat16(x); return *reinterpret_cast<unsigned short*>(&b); }

__global__ __launch_bounds__(256) void k1(const u16* x, const u16* wg, const u16* wu, u16* h){
  int wave = blockIdx.x*4 + (threadIdx.x>>6);
  int lane = threadIdx.x&63;
  int ft = wave % 112, st = wave / 112;
  int r = lane&31, hh = lane>>5;
  const u16* w = (r<16)? wg : wu;
  int f = ft*16 + (r&15);
  f32x16 acc = {};
  for(int k0=0;k0<D;k0+=16){
    bf16x8 a,b;
    for(int j=0;j<8;++j) a[j] = (short)w[(k0+8*hh+j)*F + f];
    b = *(const bf16x8*)(x + (st*32+r)*D + k0+8*hh);
    acc = __builtin_amdgcn_mfma_f32_32x32x16_bf16(a,b,acc,0,0,0);
  }
  for(int i=0;i<8;++i){
    int row = (i&3)+8*(i>>2)+4*hh;
    h[(st*32+r)*F + ft*16+row] = f2bf(acc[i]*acc[i+8]);
  }
}
__global__ __launch_bounds__(256) void k2(const u16* h, const u16* wd, u16* out){
  int wave = blockIdx.x*4 + (threadIdx.x>>6);
  int lane = threadIdx.x&63;
  int dt = wave % 16, st = wave / 16;
  int r = lane&31, hh = lane>>5;
  f32x16 acc = {};
  for(int k0=0;k0<F;k0+=16){
    bf16x8 a,b;
    for(int j=0;j<8;++j) a[j] = (short)wd[(k0+8*hh+j)*D + dt*32+r];
    b = *(const bf16x8*)(h + (st*32+r)*F + k0+8*hh);
    acc = __builtin_amdgcn_mfma_f32_32x32x16_bf16(a,b,acc,0,0,0);
  }
  for(int i=0;i<16;++i){
    int row = (i&3)+8*(i>>2)+4*hh;
    out[(st*32+r)*D + dt*32+row] = f2bf(acc[i]);
  }
}

typedef unsigned long long u64;
typedef unsigned v4u __attribute__((ext_vector_type(4)));
__device__ __forceinline__ u64 ct(){ u64 t; asm volatile("s_memtime %0\n\ts_waitcnt lgkmcnt(0)":"=s"(t)::"memory"); return t; }
__device__ __forceinline__ void spin_ticks(u64 n){ u64 t0=__builtin_amdgcn_s_memrealtime(); while(__builtin_amdgcn_s_memrealtime()-t0 < n) __builtin_amdgcn_s_sleep(1); }
__global__ __launch_bounds__(256) void probe(const char* x, const char* wg, const char* wu, const char* wd, const char* ws){
  const int lane=threadIdx.x&63, w=threadIdx.x>>6;
  const char* b = (ws + (long)w*(2<<20));
  const char* p = b + (long)blockIdx.x*7168 + lane*16;
  asm volatile(""::"v"(p));
  __builtin_amdgcn_s_barrier();
  u64 a=ct();
  v4u r[7];
  #pragma unroll
  for(int i=0;i<7;++i) asm volatile("global_load_dwordx4 %0, %1, off nt":"=v"(r[i]):"v"(p+1024*i):"memory");
  asm volatile("s_waitcnt vmcnt(0)":::"memory");
  #pragma unroll
  for(int i=0;i<7;++i) asm volatile(""::"v"(r[i]));
  __builtin_amdgcn_s_barrier();
  u64 c=ct();
  if(blockIdx.x==128 || 128<0) spin_ticks((c-a)*10);
}
extern "C" void kernel_launch(void* const* d_in, const int* in_sizes, int n_in,
                              void* d_out, int out_size, void* d_ws, size_t ws_size,
                              hipStream_t stream) {
  const u16* x=(const u16*)d_in[0]; const u16* wg=(const u16*)d_in[1]; const u16* wu=(const u16*)d_in[2]; const u16* wd=(const u16*)d_in[3];
  u16* h=(u16*)d_ws;
  probe<<<256,256,0,stream>>>((const char*)x,(const char*)wg,(const char*)wu,(const char*)wd,(const char*)d_ws + (128<<20));
  k1<<<64*112/4,256,0,stream>>>(x,wg,wu,h);
  k2<<<64*16/4,256,0,stream>>>(h,wd,(u16*)d_out);
}
```

```cpp
#include <hip/hip_runtime.h>
__global__ __launch_bounds__(256,1) void k_glu(const void* x, const void* wg, const void* wu, const void* wd, void* out, void* ws){
  __shared__ __attribute__((aligned(16))) char lds[163840];
  auto ka=__builtin_amdgcn_kernarg_segment_ptr();
  unsigned bid=blockIdx.x, tid=threadIdx.x, lb=(unsigned)(size_t)lds;
  asm volatile(R"ASM(
s_mov_b64 s[0:1], %0
s_mov_b32 s2, %1
v_mov_b32 v0, %2
s_load_dwordx8 s[4:11], s[0:1], 0
s_load_dwordx4 s[12:15], s[0:1], 32
v_lshrrev_b32 v32, 6, v0
v_and_b32 v0, 63, v0
v_readfirstlane_b32 s44, v32
s_and_b32 s60, s2, 7
s_lshr_b32 s61, s2, 3
s_mov_b32 s18, 2097152
s_mov_b32 s19, 0x20000
s_mov_b32 s22, 1835008
s_mov_b32 s23, 0x20000
s_mov_b32 s38, 16777216
s_mov_b32 s39, 0x20000
s_cmp_lt_u32 s61, 28
s_cbranch_scc1 .L_nopf_%=
s_getpc_b64 s[40:41]
.L_pc_%=:
s_and_b32 s41, s41, 0xffff
s_mov_b32 s42, .L_end_%=-.L_pc_%=
s_mov_b32 s43, 0x20000
v_lshlrev_b32 v33, 7, v0
s_lshl_b32 s62, s44, 13
buffer_load_dword v31, v33, s[40:43], s62 offen
.L_nopf_%=:
v_lshrrev_b32 v32, 3, v0
v_and_b32 v33, 7, v0
v_lshrrev_b32 v34, 4, v0
v_xor_b32 v35, v33, v34
v_lshlrev_b32 v35, 4, v35
v_lshl_add_u32 v2, v32, 10, v35
v_xor_b32 v36, 64, v2
v_add_u32 v3, 7168, v36
v_add_u32 v4, 14336, v2
v_add_u32 v5, 21504, v36
v_and_b32 v35, 1, v34
v_lshlrev_b32 v35, 1, v35
v_xor_b32 v35, v33, v35
v_lshlrev_b32 v35, 4, v35
s_and_b32 s62, s44, 1
s_lshl_b32 s62, s62, 5
v_add_u32 v36, s62, v32
v_mul_u32_u24 v36, 3584, v36
v_add_u32 v10, v36, v35
v_xor_b32 v36, 64, v10
v_add_u32 v11, 27648, v36
v_add_u32 v12, 55296, v10
v_add_u32 v13, 82944, v36
v_cmp_eq_u32 vcc, 0, v0
v_mov_b32 v1, 0x80000000
s_mul_i32 s45, s44, 28672
v_cndmask_b32 v1, v1, v0, vcc
s_add_u32 s45, s45, 49152
s_lshl_b32 s46, s44, 12
s_mul_i32 s47, s61, 37
s_lshr_b32 s47, s47, 8
s_mul_i32 s52, s47, 7
s_sub_u32 s52, s61, s52
s_and_b32 s3, s52, 1
s_and_b32 s62, s60, 4
s_add_u32 s47, s47, s62
s_lshl2_add_u32 s47, s47, s44
s_and_b32 s62, s60, 3
s_lshl2_add_u32 s55, s47, s62
s_mul_i32 s62, s62, 7
s_add_u32 s62, s62, s52
s_lshl1_add_u32 s47, s47, s3
s_lshl_b32 s47, s47, 15
s_lshl_b32 s49, s62, 7
s_xor_b32 s34, s47, 32768
s_lshl_b32 s57, s2, 7
s_add_u32 s57, s57, 15728640
s_lshl_b32 s62, s44, 29
v_or_b32 v6, s62, v1
v_mov_b32 v9, 0
s_waitcnt lgkmcnt(0)
s_mov_b32 s36, s14
s_and_b32 s37, s15, 0xffff
s_cmp_ge_u32 s44, 2
s_cselect_b32 s20, s8, s6
s_cselect_b32 s21, s9, s7
s_and_b32 s21, s21, 0xffff
buffer_store_dword v9, v6, s[36:39], s57 offen sc1
s_mov_b32 s16, s4
s_and_b32 s17, s5, 0xffff
s_cmp_ge_u32 s2, 224
s_cbranch_scc1 .L_helper_%=
s_add_u32 m0, s46, 0
s_nop 0
buffer_load_dwordx4 v10, s[20:23], s49 offen offset:0 lds
buffer_load_dwordx4 v11, s[20:23], s49 offen offset:1024 lds
buffer_load_dwordx4 v12, s[20:23], s49 offen offset:2048 lds
buffer_load_dwordx4 v13, s[20:23], s49 offen offset:3072 lds
s_add_u32 s49, s49, 229376
s_add_u32 m0, s45, 0
s_nop 0
buffer_load_dwordx4 v2, s[16:19], s47 offen offset:0 lds
buffer_load_dwordx4 v3, s[16:19], s47 offen offset:1024 lds
buffer_load_dwordx4 v4, s[16:19], s47 offen offset:2048 lds
buffer_load_dwordx4 v5, s[16:19], s47 offen offset:3072 lds
s_mov_b32 s24, s10
s_and_b32 s25, s11, 0xffff
s_mov_b32 s28, s12
s_and_b32 s29, s13, 0xffff
s_mov_b32 s26, 1835008
s_mov_b32 s27, 0x20000
s_mov_b32 s30, 2097152
s_mov_b32 s31, 0x20000
v_lshrrev_b32 v32, 3, v0
v_and_b32 v33, 7, v0
v_lshrrev_b32 v34, 4, v0
v_and_b32 v35, 15, v0
v_bfe_u32 v36, v0, 1, 3
v_xor_b32 v36, v36, v34
v_lshlrev_b32 v36, 4, v36
v_lshl_add_u32 v36, v35, 7, v36
v_add_u32 v14, s45, v36
v_xor_b32 v15, 64, v14
v_bfe_u32 v35, v0, 3, 2
v_and_b32 v36, 3, v0
v_lshlrev_b32 v36, 3, v36
v_lshl_add_u32 v35, v35, 5, v36
v_bfe_u32 v36, v0, 2, 2
v_lshl_add_u32 v36, v34, 3, v36
v_lshl_add_u32 v16, v36, 7, v35
v_xor_b32 v17, 32, v16
v_xor_b32 v18, 64, v16
v_xor_b32 v19, 96, v16
v_lshrrev_b32 v36, 2, v0
v_lshl_add_u32 v36, v36, 7, v35
v_add_u32 v24, s45, v36
v_xor_b32 v25, 32, v24
v_xor_b32 v26, 64, v24
v_xor_b32 v27, 96, v24
v_lshlrev_b32 v35, 1, v34
v_xor_b32 v35, v33, v35
v_lshlrev_b32 v35, 4, v35
v_lshl_add_u32 v20, v32, 10, v35
v_add_u32 v21, 7168, v20
v_add_u32 v22, 14336, v20
v_add_u32 v23, 21504, v20
v_lshrrev_b32 v32, 3, v0
v_and_b32 v33, 7, v0
v_lshlrev_b32 v33, 4, v33
v_lshl_add_u32 v34, v32, 8, v33
v_lshl_add_u32 v7, s3, 12, v34
v_xor_b32 v8, 4096, v7
s_mul_i32 s62, s55, 14
s_add_u32 s52, s52, s62
s_lshl_b32 s52, s52, 13
s_add_u32 s53, s52, 57344
s_lshl_b32 s55, s55, 7
s_add_u32 s55, s55, 15761408
s_and_b32 s60, s2, 7
s_lshr_b32 s61, s2, 3
s_and_b32 s62, s60, 1
s_mul_i32 s62, s62, 28
s_add_u32 s61, s61, s62
s_lshr_b32 s51, s61, 3
s_and_b32 s61, s61, 7
s_cmp_ge_u32 s51, 3
s_addc_u32 s51, s51, 0
s_lshr_b32 s60, s60, 1
s_lshl3_add_u32 s60, s60, s61
s_lshl_b32 s51, s51, 7
s_lshl2_add_u32 s56, s60, s44
s_lshl_b32 s59, s56, 14
s_add_u32 s59, s59, s51
s_mul_i32 s54, s56, 114688
s_lshl_b32 s56, s56, 7
s_add_u32 s56, s56, 15761408
s_mul_i32 s62, s44, 458752
s_add_u32 s51, s51, s62
s_add_u32 s58, s51, 32768
s_mov_b32 s61, 0
s_cmp_ge_u32 s61, 57344
s_cselect_b32 s4, 0, s61
s_add_u32 s61, s4, 8192
s_cmp_ge_u32 s61, 57344
s_cselect_b32 s5, 0, s61
s_add_u32 s61, s5, 8192
s_cmp_ge_u32 s61, 57344
s_cselect_b32 s6, 0, s61
s_add_u32 s61, s6, 8192
s_cmp_ge_u32 s61, 57344
s_cselect_b32 s7, 0, s61
s_add_u32 s61, s7, 8192
s_cmp_ge_u32 s61, 57344
s_cselect_b32 s8, 0, s61
s_add_u32 s61, s8, 8192
s_cmp_ge_u32 s61, 57344
s_cselect_b32 s9, 0, s61
s_add_u32 s61, s9, 8192
s_cmp_ge_u32 s61, 57344
s_cselect_b32 s10, 0, s61
s_add_u32 m0, s45, 4096
s_nop 0
buffer_load_dwordx4 v2, s[16:19], s34 offen offset:0 lds
buffer_load_dwordx4 v3, s[16:19], s34 offen offset:1024 lds
buffer_load_dwordx4 v4, s[16:19], s34 offen offset:2048 lds
buffer_load_dwordx4 v5, s[16:19], s34 offen offset:3072 lds
s_add_u32 m0, s46, 16384
s_nop 0
buffer_load_dwordx4 v10, s[20:23], s49 offen offset:0 lds
buffer_load_dwordx4 v11, s[20:23], s49 offen offset:1024 lds
buffer_load_dwordx4 v12, s[20:23], s49 offen offset:2048 lds
buffer_load_dwordx4 v13, s[20:23], s49 offen offset:3072 lds
s_add_u32 s49, s49, 229376
s_add_u32 m0, s45, 8064
s_nop 0
buffer_load_dwordx4 v2, s[16:19], s47 offen offset:128 lds
buffer_load_dwordx4 v3, s[16:19], s47 offen offset:1152 lds
buffer_load_dwordx4 v4, s[16:19], s47 offen offset:2176 lds
buffer_load_dwordx4 v5, s[16:19], s47 offen offset:3200 lds
s_waitcnt vmcnt(8)
s_barrier
ds_read_b128 v[32:35], v14 offset:0
ds_read_b128 v[36:39], v14 offset:2048
ds_read_b128 v[40:43], v14 offset:4096
ds_read_b128 v[44:47], v14 offset:6144
ds_read_b64_tr_b16 v[48:49], v16 offset:0
ds_read_b64_tr_b16 v[50:51], v16 offset:512
ds_read_b64_tr_b16 v[52:53], v17 offset:0
ds_read_b64_tr_b16 v[54:55], v17 offset:512
ds_read_b64_tr_b16 v[56:57], v18 offset:0
ds_read_b64_tr_b16 v[58:59], v18 offset:512
ds_read_b64_tr_b16 v[60:61], v19 offset:0
ds_read_b64_tr_b16 v[62:63], v19 offset:512
ds_read_b64_tr_b16 v[64:65], v16 offset:8192
ds_read_b64_tr_b16 v[66:67], v16 offset:8704
ds_read_b64_tr_b16 v[68:69], v17 offset:8192
ds_read_b64_tr_b16 v[70:71], v17 offset:8704
ds_read_b64_tr_b16 v[72:73], v18 offset:8192
ds_read_b64_tr_b16 v[74:75], v18 offset:8704
ds_read_b64_tr_b16 v[76:77], v19 offset:8192
ds_read_b64_tr_b16 v[78:79], v19 offset:8704
s_waitcnt lgkmcnt(0)
v_mfma_f32_16x16x32_bf16 v[128:131], v[48:51], v[32:35], 0
ds_read_b128 v[80:83], v15 offset:0
v_mfma_f32_16x16x32_bf16 v[144:147], v[52:55], v[32:35], 0
ds_read_b128 v[84:87], v15 offset:2048
v_mfma_f32_16x16x32_bf16 v[160:163], v[56:59], v[32:35], 0
ds_read_b128 v[88:91], v15 offset:4096
s_add_u32 m0, s45, 12160
v_mfma_f32_16x16x32_bf16 v[176:179], v[60:63], v[32:35], 0
buffer_load_dwordx4 v2, s[16:19], s34 offen offset:128 lds
v_mfma_f32_16x16x32_bf16 v[192:195], v[64:67], v[32:35], 0
ds_read_b128 v[92:95], v15 offset:6144
v_mfma_f32_16x16x32_bf16 v[208:211], v[68:71], v[32:35], 0
ds_read_b64_tr_b16 v[96:97], v16 offset:4096
v_mfma_f32_16x16x32_bf16 v[224:227], v[72:75], v[32:35], 0
ds_read_b64_tr_b16 v[98:99], v16 offset:4608
v_mfma_f32_16x16x32_bf16 v[240:243], v[76:79], v[32:35], 0
buffer_load_dwordx4 v3, s[16:19], s34 offen offset:1152 lds
v_mfma_f32_16x16x32_bf16 v[244:247], v[76:79], v[36:39], 0
ds_read_b64_tr_b16 v[100:101], v17 offset:4096
v_mfma_f32_16x16x32_bf16 v[228:231], v[72:75], v[36:39], 0
ds_read_b64_tr_b16 v[102:103], v17 offset:4608
v_mfma_f32_16x16x32_bf16 v[212:215], v[68:71], v[36:39], 0
ds_read_b64_tr_b16 v[104:105], v18 offset:4096
v_mfma_f32_16x16x32_bf16 v[196:199], v[64:67], v[36:39], 0
buffer_load_dwordx4 v4, s[16:19], s34 offen offset:2176 lds
v_mfma_f32_16x16x32_bf16 v[180:183], v[60:63], v[36:39], 0
ds_read_b64_tr_b16 v[106:107], v18 offset:4608
v_mfma_f32_16x16x32_bf16 v[164:167], v[56:59], v[36:39], 0
ds_read_b64_tr_b16 v[108:109], v19 offset:4096
v_mfma_f32_16x16x32_bf16 v[148:151], v[52:55], v[36:39], 0
ds_read_b64_tr_b16 v[110:111], v19 offset:4608
v_mfma_f32_16x16x32_bf16 v[132:135], v[48:51], v[36:39], 0
buffer_load_dwordx4 v5, s[16:19], s34 offen offset:3200 lds
v_mfma_f32_16x16x32_bf16 v[136:139], v[48:51], v[40:43], 0
ds_read_b64_tr_b16 v[112:113], v16 offset:12288
v_mfma_f32_16x16x32_bf16 v[152:155], v[52:55], v[40:43], 0
ds_read_b64_tr_b16 v[114:115], v16 offset:12800
v_mfma_f32_16x16x32_bf16 v[168:171], v[56:59], v[40:43], 0
ds_read_b64_tr_b16 v[116:117], v17 offset:12288
s_add_u32 m0, s46, 32768
v_mfma_f32_16x16x32_bf16 v[184:187], v[60:63], v[40:43], 0
buffer_load_dwordx4 v10, s[20:23], s49 offen offset:0 lds
v_mfma_f32_16x16x32_bf16 v[200:203], v[64:67], v[40:43], 0
ds_read_b64_tr_b16 v[118:119], v17 offset:12800
v_mfma_f32_16x16x32_bf16 v[216:219], v[68:71], v[40:43], 0
ds_read_b64_tr_b16 v[120:121], v18 offset:12288
v_mfma_f32_16x16x32_bf16 v[232:235], v[72:75], v[40:43], 0
ds_read_b64_tr_b16 v[122:123], v18 offset:12800
v_mfma_f32_16x16x32_bf16 v[248:251], v[76:79], v[40:43], 0
buffer_load_dwordx4 v11, s[20:23], s49 offen offset:1024 lds
v_mfma_f32_16x16x32_bf16 v[252:255], v[76:79], v[44:47], 0
ds_read_b64_tr_b16 v[124:125], v19 offset:12288
v_mfma_f32_16x16x32_bf16 v[236:239], v[72:75], v[44:47], 0
ds_read_b64_tr_b16 v[126:127], v19 offset:12800
v_mfma_f32_16x16x32_bf16 v[220:223], v[68:71], v[44:47], 0
v_mfma_f32_16x16x32_bf16 v[204:207], v[64:67], v[44:47], 0
buffer_load_dwordx4 v12, s[20:23], s49 offen offset:2048 lds
v_mfma_f32_16x16x32_bf16 v[188:191], v[60:63], v[44:47], 0
v_mfma_f32_16x16x32_bf16 v[172:175], v[56:59], v[44:47], 0
v_mfma_f32_16x16x32_bf16 v[156:159], v[52:55], v[44:47], 0
v_mfma_f32_16x16x32_bf16 v[140:143], v[48:51], v[44:47], 0
buffer_load_dwordx4 v13, s[20:23], s49 offen offset:3072 lds
s_waitcnt lgkmcnt(0)
v_mfma_f32_16x16x32_bf16 v[128:131], v[96:99], v[80:83], v[128:131]
s_add_u32 s49, s49, 229376
s_waitcnt vmcnt(4)
s_barrier
v_mfma_f32_16x16x32_bf16 v[144:147], v[100:103], v[80:83], v[144:147]
ds_read_b128 v[32:35], v14 offset:8192
v_mfma_f32_16x16x32_bf16 v[160:163], v[104:107], v[80:83], v[160:163]
ds_read_b128 v[36:39], v14 offset:10240
s_add_u32 m0, s45, 16128
v_mfma_f32_16x16x32_bf16 v[176:179], v[108:111], v[80:83], v[176:179]
buffer_load_dwordx4 v2, s[16:19], s47 offen offset:256 lds
v_mfma_f32_16x16x32_bf16 v[192:195], v[112:115], v[80:83], v[192:195]
ds_read_b128 v[40:43], v14 offset:12288
v_mfma_f32_16x16x32_bf16 v[208:211], v[116:119], v[80:83], v[208:211]
ds_read_b128 v[44:47], v14 offset:14336
v_mfma_f32_16x16x32_bf16 v[224:227], v[120:123], v[80:83], v[224:227]
ds_read_b64_tr_b16 v[48:49], v16 offset:16384
v_mfma_f32_16x16x32_bf16 v[240:243], v[124:127], v[80:83], v[240:243]
buffer_load_dwordx4 v3, s[16:19], s47 offen offset:1280 lds
v_mfma_f32_16x16x32_bf16 v[244:247], v[124:127], v[84:87], v[244:247]
ds_read_b64_tr_b16 v[50:51], v16 offset:16896
v_mfma_f32_16x16x32_bf16 v[228:231], v[120:123], v[84:87], v[228:231]
ds_read_b64_tr_b16 v[52:53], v17 offset:16384
v_mfma_f32_16x16x32_bf16 v[212:215], v[116:119], v[84:87], v[212:215]
ds_read_b64_tr_b16 v[54:55], v17 offset:16896
v_mfma_f32_16x16x32_bf16 v[196:199], v[112:115], v[84:87], v[196:199]
buffer_load_dwordx4 v4, s[16:19], s47 offen offset:2304 lds
v_mfma_f32_16x16x32_bf16 v[180:183], v[108:111], v[84:87], v[180:183]
ds_read_b64_tr_b16 v[56:57], v18 offset:16384
v_mfma_f32_16x16x32_bf16 v[164:167], v[104:107], v[84:87], v[164:167]
ds_read_b64_tr_b16 v[58:59], v18 offset:16896
v_mfma_f32_16x16x32_bf16 v[148:151], v[100:103], v[84:87], v[148:151]
ds_read_b64_tr_b16 v[60:61], v19 offset:16384
v_mfma_f32_16x16x32_bf16 v[132:135], v[96:99], v[84:87], v[132:135]
buffer_load_dwordx4 v5, s[16:19], s47 offen offset:3328 lds
v_mfma_f32_16x16x32_bf16 v[136:139], v[96:99], v[88:91], v[136:139]
ds_read_b64_tr_b16 v[62:63], v19 offset:16896
v_mfma_f32_16x16x32_bf16 v[152:155], v[100:103], v[88:91], v[152:155]
ds_read_b64_tr_b16 v[64:65], v16 offset:24576
v_mfma_f32_16x16x32_bf16 v[168:171], v[104:107], v[88:91], v[168:171]
ds_read_b64_tr_b16 v[66:67], v16 offset:25088
s_add_u32 m0, s45, 20224
v_mfma_f32_16x16x32_bf16 v[184:187], v[108:111], v[88:91], v[184:187]
buffer_load_dwordx4 v2, s[16:19], s34 offen offset:256 lds
v_mfma_f32_16x16x32_bf16 v[200:203], v[112:115], v[88:91], v[200:203]
ds_read_b64_tr_b16 v[68:69], v17 offset:24576
v_mfma_f32_16x16x32_bf16 v[216:219], v[116:119], v[88:91], v[216:219]
ds_read_b64_tr_b16 v[70:71], v17 offset:25088
v_mfma_f32_16x16x32_bf16 v[232:235], v[120:123], v[88:91], v[232:235]
ds_read_b64_tr_b16 v[72:73], v18 offset:24576
v_mfma_f32_16x16x32_bf16 v[248:251], v[124:127], v[88:91], v[248:251]
buffer_load_dwordx4 v3, s[16:19], s34 offen offset:1280 lds
v_mfma_f32_16x16x32_bf16 v[252:255], v[124:127], v[92:95], v[252:255]
ds_read_b64_tr_b16 v[74:75], v18 offset:25088
v_mfma_f32_16x16x32_bf16 v[236:239], v[120:123], v[92:95], v[236:239]
ds_read_b64_tr_b16 v[76:77], v19 offset:24576
v_mfma_f32_16x16x32_bf16 v[220:223], v[116:119], v[92:95], v[220:223]
ds_read_b64_tr_b16 v[78:79], v19 offset:25088
v_mfma_f32_16x16x32_bf16 v[204:207], v[112:115], v[92:95], v[204:207]
buffer_load_dwordx4 v4, s[16:19], s34 offen offset:2304 lds
v_mfma_f32_16x16x32_bf16 v[188:191], v[108:111], v[92:95], v[188:191]
v_mfma_f32_16x16x32_bf16 v[172:175], v[104:107], v[92:95], v[172:175]
v_mfma_f32_16x16x32_bf16 v[156:159], v[100:103], v[92:95], v[156:159]
v_mfma_f32_16x16x32_bf16 v[140:143], v[96:99], v[92:95], v[140:143]
buffer_load_dwordx4 v5, s[16:19], s34 offen offset:3328 lds
s_waitcnt lgkmcnt(0)
v_mfma_f32_16x16x32_bf16 v[128:131], v[48:51], v[32:35], v[128:131]
ds_read_b128 v[80:83], v15 offset:8192
v_mfma_f32_16x16x32_bf16 v[144:147], v[52:55], v[32:35], v[144:147]
ds_read_b128 v[84:87], v15 offset:10240
v_mfma_f32_16x16x32_bf16 v[160:163], v[56:59], v[32:35], v[160:163]
ds_read_b128 v[88:91], v15 offset:12288
s_add_u32 m0, s46, 0
v_mfma_f32_16x16x32_bf16 v[176:179], v[60:63], v[32:35], v[176:179]
buffer_load_dwordx4 v10, s[20:23], s49 offen offset:0 lds
v_mfma_f32_16x16x32_bf16 v[192:195], v[64:67], v[32:35], v[192:195]
ds_read_b128 v[92:95], v15 offset:14336
v_mfma_f32_16x16x32_bf16 v[208:211], v[68:71], v[32:35], v[208:211]
ds_read_b64_tr_b16 v[96:97], v16 offset:20480
v_mfma_f32_16x16x32_bf16 v[224:227], v[72:75], v[32:35], v[224:227]
ds_read_b64_tr_b16 v[98:99], v16 offset:20992
v_mfma_f32_16x16x32_bf16 v[240:243], v[76:79], v[32:35], v[240:243]
buffer_load_dwordx4 v11, s[20:23], s49 offen offset:1024 lds
v_mfma_f32_16x16x32_bf16 v[244:247], v[76:79], v[36:39], v[244:247]
ds_read_b64_tr_b16 v[100:101], v17 offset:20480
v_mfma_f32_16x16x32_bf16 v[228:231], v[72:75], v[36:39], v[228:231]
ds_read_b64_tr_b16 v[102:103], v17 offset:20992
v_mfma_f32_16x16x32_bf16 v[212:215], v[68:71], v[36:39], v[212:215]
ds_read_b64_tr_b16 v[104:105], v18 offset:20480
v_mfma_f32_16x16x32_bf16 v[196:199], v[64:67], v[36:39], v[196:199]
buffer_load_dwordx4 v12, s[20:23], s49 offen offset:2048 lds
v_mfma_f32_16x16x32_bf16 v[180:183], v[60:63], v[36:39], v[180:183]
ds_read_b64_tr_b16 v[106:107], v18 offset:20992
v_mfma_f32_16x16x32_bf16 v[164:167], v[56:59], v[36:39], v[164:167]
ds_read_b64_tr_b16 v[108:109], v19 offset:20480
v_mfma_f32_16x16x32_bf16 v[148:151], v[52:55], v[36:39], v[148:151]
ds_read_b64_tr_b16 v[110:111], v19 offset:20992
v_mfma_f32_16x16x32_bf16 v[132:135], v[48:51], v[36:39], v[132:135]
buffer_load_dwordx4 v13, s[20:23], s49 offen offset:3072 lds
v_mfma_f32_16x16x32_bf16 v[136:139], v[48:51], v[40:43], v[136:139]
ds_read_b64_tr_b16 v[112:113], v16 offset:28672
s_add_u32 s49, s49, 229376
v_mfma_f32_16x16x32_bf16 v[152:155], v[52:55], v[40:43], v[152:155]
ds_read_b64_tr_b16 v[114:115], v16 offset:29184
v_mfma_f32_16x16x32_bf16 v[168:171], v[56:59], v[40:43], v[168:171]
ds_read_b64_tr_b16 v[116:117], v17 offset:28672
s_add_u32 m0, s45, -384
v_mfma_f32_16x16x32_bf16 v[184:187], v[60:63], v[40:43], v[184:187]
buffer_load_dwordx4 v2, s[16:19], s47 offen offset:384 lds
v_mfma_f32_16x16x32_bf16 v[200:203], v[64:67], v[40:43], v[200:203]
ds_read_b64_tr_b16 v[118:119], v17 offset:29184
v_mfma_f32_16x16x32_bf16 v[216:219], v[68:71], v[40:43], v[216:219]
ds_read_b64_tr_b16 v[120:121], v18 offset:28672
v_mfma_f32_16x16x32_bf16 v[232:235], v[72:75], v[40:43], v[232:235]
ds_read_b64_tr_b16 v[122:123], v18 offset:29184
v_mfma_f32_16x16x32_bf16 v[248:251], v[76:79], v[40:43], v[248:251]
buffer_load_dwordx4 v3, s[16:19], s47 offen offset:1408 lds
v_mfma_f32_16x16x32_bf16 v[252:255], v[76:79], v[44:47], v[252:255]
ds_read_b64_tr_b16 v[124:125], v19 offset:28672
v_mfma_f32_16x16x32_bf16 v[236:239], v[72:75], v[44:47], v[236:239]
ds_read_b64_tr_b16 v[126:127], v19 offset:29184
v_mfma_f32_16x16x32_bf16 v[220:223], v[68:71], v[44:47], v[220:223]
v_mfma_f32_16x16x32_bf16 v[204:207], v[64:67], v[44:47], v[204:207]
buffer_load_dwordx4 v4, s[16:19], s47 offen offset:2432 lds
v_mfma_f32_16x16x32_bf16 v[188:191], v[60:63], v[44:47], v[188:191]
v_mfma_f32_16x16x32_bf16 v[172:175], v[56:59], v[44:47], v[172:175]
v_mfma_f32_16x16x32_bf16 v[156:159], v[52:55], v[44:47], v[156:159]
v_mfma_f32_16x16x32_bf16 v[140:143], v[48:51], v[44:47], v[140:143]
buffer_load_dwordx4 v5, s[16:19], s47 offen offset:3456 lds
s_waitcnt lgkmcnt(0)
v_mfma_f32_16x16x32_bf16 v[128:131], v[96:99], v[80:83], v[128:131]
s_waitcnt vmcnt(8)
s_barrier
v_mfma_f32_16x16x32_bf16 v[144:147], v[100:103], v[80:83], v[144:147]
ds_read_b128 v[32:35], v14 offset:16384
v_mfma_f32_16x16x32_bf16 v[160:163], v[104:107], v[80:83], v[160:163]
ds_read_b128 v[36:39], v14 offset:18432
s_add_u32 m0, s45, 3712
v_mfma_f32_16x16x32_bf16 v[176:179], v[108:111], v[80:83], v[176:179]
buffer_load_dwordx4 v2, s[16:19], s34 offen offset:384 lds
v_mfma_f32_16x16x32_bf16 v[192:195], v[112:115], v[80:83], v[192:195]
ds_read_b128 v[40:43], v14 offset:20480
v_mfma_f32_16x16x32_bf16 v[208:211], v[116:119], v[80:83], v[208:211]
ds_read_b128 v[44:47], v14 offset:22528
v_mfma_f32_16x16x32_bf16 v[224:227], v[120:123], v[80:83], v[224:227]
ds_read_b64_tr_b16 v[48:49], v16 offset:32768
v_mfma_f32_16x16x32_bf16 v[240:243], v[124:127], v[80:83], v[240:243]
buffer_load_dwordx4 v3, s[16:19], s34 offen offset:1408 lds
v_mfma_f32_16x16x32_bf16 v[244:247], v[124:127], v[84:87], v[244:247]
ds_read_b64_tr_b16 v[50:51], v16 offset:33280
v_mfma_f32_16x16x32_bf16 v[228:231], v[120:123], v[84:87], v[228:231]
ds_read_b64_tr_b16 v[52:53], v17 offset:32768
v_mfma_f32_16x16x32_bf16 v[212:215], v[116:119], v[84:87], v[212:215]
ds_read_b64_tr_b16 v[54:55], v17 offset:33280
v_mfma_f32_16x16x32_bf16 v[196:199], v[112:115], v[84:87], v[196:199]
buffer_load_dwordx4 v4, s[16:19], s34 offen offset:2432 lds
v_mfma_f32_16x16x32_bf16 v[180:183], v[108:111], v[84:87], v[180:183]
ds_read_b64_tr_b16 v[56:57], v18 offset:32768
v_mfma_f32_16x16x32_bf16 v[164:167], v[104:107], v[84:87], v[164:167]
ds_read_b64_tr_b16 v[58:59], v18 offset:33280
v_mfma_f32_16x16x32_bf16 v[148:151], v[100:103], v[84:87], v[148:151]
ds_read_b64_tr_b16 v[60:61], v19 offset:32768
v_mfma_f32_16x16x32_bf16 v[132:135], v[96:99], v[84:87], v[132:135]
buffer_load_dwordx4 v5, s[16:19], s34 offen offset:3456 lds
v_mfma_f32_16x16x32_bf16 v[136:139], v[96:99], v[88:91], v[136:139]
ds_read_b64_tr_b16 v[62:63], v19 offset:33280
v_mfma_f32_16x16x32_bf16 v[152:155], v[100:103], v[88:91], v[152:155]
ds_read_b64_tr_b16 v[64:65], v16 offset:40960
v_mfma_f32_16x16x32_bf16 v[168:171], v[104:107], v[88:91], v[168:171]
ds_read_b64_tr_b16 v[66:67], v16 offset:41472
s_add_u32 m0, s46, 16384
v_mfma_f32_16x16x32_bf16 v[184:187], v[108:111], v[88:91], v[184:187]
buffer_load_dwordx4 v10, s[20:23], s49 offen offset:0 lds
v_mfma_f32_16x16x32_bf16 v[200:203], v[112:115], v[88:91], v[200:203]
ds_read_b64_tr_b16 v[68:69], v17 offset:40960
v_mfma_f32_16x16x32_bf16 v[216:219], v[116:119], v[88:91], v[216:219]
ds_read_b64_tr_b16 v[70:71], v17 offset:41472
v_mfma_f32_16x16x32_bf16 v[232:235], v[120:123], v[88:91], v[232:235]
ds_read_b64_tr_b16 v[72:73], v18 offset:40960
v_mfma_f32_16x16x32_bf16 v[248:251], v[124:127], v[88:91], v[248:251]
buffer_load_dwordx4 v11, s[20:23], s49 offen offset:1024 lds
v_mfma_f32_16x16x32_bf16 v[252:255], v[124:127], v[92:95], v[252:255]
ds_read_b64_tr_b16 v[74:75], v18 offset:41472
v_mfma_f32_16x16x32_bf16 v[236:239], v[120:123], v[92:95], v[236:239]
ds_read_b64_tr_b16 v[76:77], v19 offset:40960
v_mfma_f32_16x16x32_bf16 v[220:223], v[116:119], v[92:95], v[220:223]
ds_read_b64_tr_b16 v[78:79], v19 offset:41472
v_mfma_f32_16x16x32_bf16 v[204:207], v[112:115], v[92:95], v[204:207]
buffer_load_dwordx4 v12, s[20:23], s49 offen offset:2048 lds
v_mfma_f32_16x16x32_bf16 v[188:191], v[108:111], v[92:95], v[188:191]
v_mfma_f32_16x16x32_bf16 v[172:175], v[104:107], v[92:95], v[172:175]
v_mfma_f32_16x16x32_bf16 v[156:159], v[100:103], v[92:95], v[156:159]
v_mfma_f32_16x16x32_bf16 v[140:143], v[96:99], v[92:95], v[140:143]
buffer_load_dwordx4 v13, s[20:23], s49 offen offset:3072 lds
s_waitcnt lgkmcnt(0)
v_mfma_f32_16x16x32_bf16 v[128:131], v[48:51], v[32:35], v[128:131]
ds_read_b128 v[80:83], v15 offset:16384
s_add_u32 s49, s49, 229376
v_mfma_f32_16x16x32_bf16 v[144:147], v[52:55], v[32:35], v[144:147]
ds_read_b128 v[84:87], v15 offset:18432
v_mfma_f32_16x16x32_bf16 v[160:163], v[56:59], v[32:35], v[160:163]
ds_read_b128 v[88:91], v15 offset:20480
s_add_u32 m0, s45, 7680
v_mfma_f32_16x16x32_bf16 v[176:179], v[60:63], v[32:35], v[176:179]
buffer_load_dwordx4 v2, s[16:19], s47 offen offset:512 lds
v_mfma_f32_16x16x32_bf16 v[192:195], v[64:67], v[32:35], v[192:195]
ds_read_b128 v[92:95], v15 offset:22528
v_mfma_f32_16x16x32_bf16 v[208:211], v[68:71], v[32:35], v[208:211]
ds_read_b64_tr_b16 v[96:97], v16 offset:36864
v_mfma_f32_16x16x32_bf16 v[224:227], v[72:75], v[32:35], v[224:227]
ds_read_b64_tr_b16 v[98:99], v16 offset:37376
v_mfma_f32_16x16x32_bf16 v[240:243], v[76:79], v[32:35], v[240:243]
buffer_load_dwordx4 v3, s[16:19], s47 offen offset:1536 lds
v_mfma_f32_16x16x32_bf16 v[244:247], v[76:79], v[36:39], v[244:247]
ds_read_b64_tr_b16 v[100:101], v17 offset:36864
v_mfma_f32_16x16x32_bf16 v[228:231], v[72:75], v[36:39], v[228:231]
ds_read_b64_tr_b16 v[102:103], v17 offset:37376
v_mfma_f32_16x16x32_bf16 v[212:215], v[68:71], v[36:39], v[212:215]
ds_read_b64_tr_b16 v[104:105], v18 offset:36864
v_mfma_f32_16x16x32_bf16 v[196:199], v[64:67], v[36:39], v[196:199]
buffer_load_dwordx4 v4, s[16:19], s47 offen offset:2560 lds
v_mfma_f32_16x16x32_bf16 v[180:183], v[60:63], v[36:39], v[180:183]
ds_read_b64_tr_b16 v[106:107], v18 offset:37376
v_mfma_f32_16x16x32_bf16 v[164:167], v[56:59], v[36:39], v[164:167]
ds_read_b64_tr_b16 v[108:109], v19 offset:36864
v_mfma_f32_16x16x32_bf16 v[148:151], v[52:55], v[36:39], v[148:151]
ds_read_b64_tr_b16 v[110:111], v19 offset:37376
v_mfma_f32_16x16x32_bf16 v[132:135], v[48:51], v[36:39], v[132:135]
buffer_load_dwordx4 v5, s[16:19], s47 offen offset:3584 lds
v_mfma_f32_16x16x32_bf16 v[136:139], v[48:51], v[40:43], v[136:139]
ds_read_b64_tr_b16 v[112:113], v16 offset:45056
v_mfma_f32_16x16x32_bf16 v[152:155], v[52:55], v[40:43], v[152:155]
ds_read_b64_tr_b16 v[114:115], v16 offset:45568
v_mfma_f32_16x16x32_bf16 v[168:171], v[56:59], v[40:43], v[168:171]
ds_read_b64_tr_b16 v[116:117], v17 offset:45056
s_add_u32 m0, s45, 11776
v_mfma_f32_16x16x32_bf16 v[184:187], v[60:63], v[40:43], v[184:187]
buffer_load_dwordx4 v2, s[16:19], s34 offen offset:512 lds
v_mfma_f32_16x16x32_bf16 v[200:203], v[64:67], v[40:43], v[200:203]
ds_read_b64_tr_b16 v[118:119], v17 offset:45568
v_mfma_f32_16x16x32_bf16 v[216:219], v[68:71], v[40:43], v[216:219]
ds_read_b64_tr_b16 v[120:121], v18 offset:45056
v_mfma_f32_16x16x32_bf16 v[232:235], v[72:75], v[40:43], v[232:235]
ds_read_b64_tr_b16 v[122:123], v18 offset:45568
v_mfma_f32_16x16x32_bf16 v[248:251], v[76:79], v[40:43], v[248:251]
buffer_load_dwordx4 v3, s[16:19], s34 offen offset:1536 lds
v_mfma_f32_16x16x32_bf16 v[252:255], v[76:79], v[44:47], v[252:255]
ds_read_b64_tr_b16 v[124:125], v19 offset:45056
v_mfma_f32_16x16x32_bf16 v[236:239], v[72:75], v[44:47], v[236:239]
ds_read_b64_tr_b16 v[126:127], v19 offset:45568
v_mfma_f32_16x16x32_bf16 v[220:223], v[68:71], v[44:47], v[220:223]
v_mfma_f32_16x16x32_bf16 v[204:207], v[64:67], v[44:47], v[204:207]
buffer_load_dwordx4 v4, s[16:19], s34 offen offset:2560 lds
v_mfma_f32_16x16x32_bf16 v[188:191], v[60:63], v[44:47], v[188:191]
v_mfma_f32_16x16x32_bf16 v[172:175], v[56:59], v[44:47], v[172:175]
v_mfma_f32_16x16x32_bf16 v[156:159], v[52:55], v[44:47], v[156:159]
v_mfma_f32_16x16x32_bf16 v[140:143], v[48:51], v[44:47], v[140:143]
buffer_load_dwordx4 v5, s[16:19], s34 offen offset:3584 lds
s_waitcnt lgkmcnt(0)
v_mfma_f32_16x16x32_bf16 v[128:131], v[96:99], v[80:83], v[128:131]
s_waitcnt vmcnt(12)
s_barrier
v_mfma_f32_16x16x32_bf16 v[144:147], v[100:103], v[80:83], v[144:147]
ds_read_b128 v[32:35], v14 offset:0
v_mfma_f32_16x16x32_bf16 v[160:163], v[104:107], v[80:83], v[160:163]
ds_read_b128 v[36:39], v14 offset:2048
s_add_u32 m0, s46, 32768
v_mfma_f32_16x16x32_bf16 v[176:179], v[108:111], v[80:83], v[176:179]
buffer_load_dwordx4 v10, s[20:23], s49 offen offset:0 lds
v_mfma_f32_16x16x32_bf16 v[192:195], v[112:115], v[80:83], v[192:195]
ds_read_b128 v[40:43], v14 offset:4096
v_mfma_f32_16x16x32_bf16 v[208:211], v[116:119], v[80:83], v[208:211]
ds_read_b128 v[44:47], v14 offset:6144
v_mfma_f32_16x16x32_bf16 v[224:227], v[120:123], v[80:83], v[224:227]
ds_read_b64_tr_b16 v[48:49], v16 offset:0
v_mfma_f32_16x16x32_bf16 v[240:243], v[124:127], v[80:83], v[240:243]
buffer_load_dwordx4 v11, s[20:23], s49 offen offset:1024 lds
v_mfma_f32_16x16x32_bf16 v[244:247], v[124:127], v[84:87], v[244:247]
ds_read_b64_tr_b16 v[50:51], v16 offset:512
v_mfma_f32_16x16x32_bf16 v[228:231], v[120:123], v[84:87], v[228:231]
ds_read_b64_tr_b16 v[52:53], v17 offset:0
v_mfma_f32_16x16x32_bf16 v[212:215], v[116:119], v[84:87], v[212:215]
ds_read_b64_tr_b16 v[54:55], v17 offset:512
v_mfma_f32_16x16x32_bf16 v[196:199], v[112:115], v[84:87], v[196:199]
buffer_load_dwordx4 v12, s[20:23], s49 offen offset:2048 lds
v_mfma_f32_16x16x32_bf16 v[180:183], v[108:111], v[84:87], v[180:183]
ds_read_b64_tr_b16 v[56:57], v18 offset:0
v_mfma_f32_16x16x32_bf16 v[164:167], v[104:107], v[84:87], v[164:167]
ds_read_b64_tr_b16 v[58:59], v18 offset:512
v_mfma_f32_16x16x32_bf16 v[148:151], v[100:103], v[84:87], v[148:151]
ds_read_b64_tr_b16 v[60:61], v19 offset:0
v_mfma_f32_16x16x32_bf16 v[132:135], v[96:99], v[84:87], v[132:135]
buffer_load_dwordx4 v13, s[20:23], s49 offen offset:3072 lds
v_mfma_f32_16x16x32_bf16 v[136:139], v[96:99], v[88:91], v[136:139]
ds_read_b64_tr_b16 v[62:63], v19 offset:512
s_add_u32 s49, s49, 229376
v_mfma_f32_16x16x32_bf16 v[152:155], v[100:103], v[88:91], v[152:155]
ds_read_b64_tr_b16 v[64:65], v16 offset:8192
v_mfma_f32_16x16x32_bf16 v[168:171], v[104:107], v[88:91], v[168:171]
ds_read_b64_tr_b16 v[66:67], v16 offset:8704
s_add_u32 m0, s45, 15744
v_mfma_f32_16x16x32_bf16 v[184:187], v[108:111], v[88:91], v[184:187]
buffer_load_dwordx4 v2, s[16:19], s47 offen offset:640 lds
v_mfma_f32_16x16x32_bf16 v[200:203], v[112:115], v[88:91], v[200:203]
ds_read_b64_tr_b16 v[68:69], v17 offset:8192
v_mfma_f32_16x16x32_bf16 v[216:219], v[116:119], v[88:91], v[216:219]
ds_read_b64_tr_b16 v[70:71], v17 offset:8704
v_mfma_f32_16x16x32_bf16 v[232:235], v[120:123], v[88:91], v[232:235]
ds_read_b64_tr_b16 v[72:73], v18 offset:8192
v_mfma_f32_16x16x32_bf16 v[248:251], v[124:127], v[88:91], v[248:251]
buffer_load_dwordx4 v3, s[16:19], s47 offen offset:1664 lds
v_mfma_f32_16x16x32_bf16 v[252:255], v[124:127], v[92:95], v[252:255]
ds_read_b64_tr_b16 v[74:75], v18 offset:8704
v_mfma_f32_16x16x32_bf16 v[236:239], v[120:123], v[92:95], v[236:239]
ds_read_b64_tr_b16 v[76:77], v19 offset:8192
v_mfma_f32_16x16x32_bf16 v[220:223], v[116:119], v[92:95], v[220:223]
ds_read_b64_tr_b16 v[78:79], v19 offset:8704
v_mfma_f32_16x16x32_bf16 v[204:207], v[112:115], v[92:95], v[204:207]
buffer_load_dwordx4 v4, s[16:19], s47 offen offset:2688 lds
v_mfma_f32_16x16x32_bf16 v[188:191], v[108:111], v[92:95], v[188:191]
v_mfma_f32_16x16x32_bf16 v[172:175], v[104:107], v[92:95], v[172:175]
v_mfma_f32_16x16x32_bf16 v[156:159], v[100:103], v[92:95], v[156:159]
v_mfma_f32_16x16x32_bf16 v[140:143], v[96:99], v[92:95], v[140:143]
buffer_load_dwordx4 v5, s[16:19], s47 offen offset:3712 lds
s_waitcnt lgkmcnt(0)
v_mfma_f32_16x16x32_bf16 v[128:131], v[48:51], v[32:35], v[128:131]
ds_read_b128 v[80:83], v15 offset:0
v_mfma_f32_16x16x32_bf16 v[144:147], v[52:55], v[32:35], v[144:147]
ds_read_b128 v[84:87], v15 offset:2048
v_mfma_f32_16x16x32_bf16 v[160:163], v[56:59], v[32:35], v[160:163]
ds_read_b128 v[88:91], v15 offset:4096
s_add_u32 m0, s45, 19840
v_mfma_f32_16x16x32_bf16 v[176:179], v[60:63], v[32:35], v[176:179]
buffer_load_dwordx4 v2, s[16:19], s34 offen offset:640 lds
v_mfma_f32_16x16x32_bf16 v[192:195], v[64:67], v[32:35], v[192:195]
ds_read_b128 v[92:95], v15 offset:6144
v_mfma_f32_16x16x32_bf16 v[208:211], v[68:71], v[32:35], v[208:211]
ds_read_b64_tr_b16 v[96:97], v16 offset:4096
v_mfma_f32_16x16x32_bf16 v[224:227], v[72:75], v[32:35], v[224:227]
ds_read_b64_tr_b16 v[98:99], v16 offset:4608
v_mfma_f32_16x16x32_bf16 v[240:243], v[76:79], v[32:35], v[240:243]
buffer_load_dwordx4 v3, s[16:19], s34 offen offset:1664 lds
v_mfma_f32_16x16x32_bf16 v[244:247], v[76:79], v[36:39], v[244:247]
ds_read_b64_tr_b16 v[100:101], v17 offset:4096
v_mfma_f32_16x16x32_bf16 v[228:231], v[72:75], v[36:39], v[228:231]
ds_read_b64_tr_b16 v[102:103], v17 offset:4608
v_mfma_f32_16x16x32_bf16 v[212:215], v[68:71], v[36:39], v[212:215]
ds_read_b64_tr_b16 v[104:105], v18 offset:4096
v_mfma_f32_16x16x32_bf16 v[196:199], v[64:67], v[36:39], v[196:199]
buffer_load_dwordx4 v4, s[16:19], s34 offen offset:2688 lds
v_mfma_f32_16x16x32_bf16 v[180:183], v[60:63], v[36:39], v[180:183]
ds_read_b64_tr_b16 v[106:107], v18 offset:4608
v_mfma_f32_16x16x32_bf16 v[164:167], v[56:59], v[36:39], v[164:167]
ds_read_b64_tr_b16 v[108:109], v19 offset:4096
v_mfma_f32_16x16x32_bf16 v[148:151], v[52:55], v[36:39], v[148:151]
ds_read_b64_tr_b16 v[110:111], v19 offset:4608
v_mfma_f32_16x16x32_bf16 v[132:135], v[48:51], v[36:39], v[132:135]
buffer_load_dwordx4 v5, s[16:19], s34 offen offset:3712 lds
v_mfma_f32_16x16x32_bf16 v[136:139], v[48:51], v[40:43], v[136:139]
ds_read_b64_tr_b16 v[112:113], v16 offset:12288
v_mfma_f32_16x16x32_bf16 v[152:155], v[52:55], v[40:43], v[152:155]
ds_read_b64_tr_b16 v[114:115], v16 offset:12800
v_mfma_f32_16x16x32_bf16 v[168:171], v[56:59], v[40:43], v[168:171]
ds_read_b64_tr_b16 v[116:117], v17 offset:12288
v_mfma_f32_16x16x32_bf16 v[184:187], v[60:63], v[40:43], v[184:187]
v_mfma_f32_16x16x32_bf16 v[200:203], v[64:67], v[40:43], v[200:203]
ds_read_b64_tr_b16 v[118:119], v17 offset:12800
v_mfma_f32_16x16x32_bf16 v[216:219], v[68:71], v[40:43], v[216:219]
ds_read_b64_tr_b16 v[120:121], v18 offset:12288
v_mfma_f32_16x16x32_bf16 v[232:235], v[72:75], v[40:43], v[232:235]
ds_read_b64_tr_b16 v[122:123], v18 offset:12800
v_mfma_f32_16x16x32_bf16 v[248:251], v[76:79], v[40:43], v[248:251]
v_mfma_f32_16x16x32_bf16 v[252:255], v[76:79], v[44:47], v[252:255]
ds_read_b64_tr_b16 v[124:125], v19 offset:12288
v_mfma_f32_16x16x32_bf16 v[236:239], v[72:75], v[44:47], v[236:239]
ds_read_b64_tr_b16 v[126:127], v19 offset:12800
v_mfma_f32_16x16x32_bf16 v[220:223], v[68:71], v[44:47], v[220:223]
v_mfma_f32_16x16x32_bf16 v[204:207], v[64:67], v[44:47], v[204:207]
v_mfma_f32_16x16x32_bf16 v[188:191], v[60:63], v[44:47], v[188:191]
v_mfma_f32_16x16x32_bf16 v[172:175], v[56:59], v[44:47], v[172:175]
v_mfma_f32_16x16x32_bf16 v[156:159], v[52:55], v[44:47], v[156:159]
v_mfma_f32_16x16x32_bf16 v[140:143], v[48:51], v[44:47], v[140:143]
s_waitcnt lgkmcnt(0)
v_mfma_f32_16x16x32_bf16 v[128:131], v[96:99], v[80:83], v[128:131]
s_waitcnt vmcnt(12)
s_barrier
v_mfma_f32_16x16x32_bf16 v[144:147], v[100:103], v[80:83], v[144:147]
ds_read_b128 v[32:35], v14 offset:8192
v_mfma_f32_16x16x32_bf16 v[160:163], v[104:107], v[80:83], v[160:163]
ds_read_b128 v[36:39], v14 offset:10240
s_add_u32 m0, s46, 0
v_mfma_f32_16x16x32_bf16 v[176:179], v[108:111], v[80:83], v[176:179]
buffer_load_dwordx4 v10, s[20:23], s49 offen offset:0 lds
v_mfma_f32_16x16x32_bf16 v[192:195], v[112:115], v[80:83], v[192:195]
ds_read_b128 v[40:43], v14 offset:12288
v_mfma_f32_16x16x32_bf16 v[208:211], v[116:119], v[80:83], v[208:211]
ds_read_b128 v[44:47], v14 offset:14336
v_mfma_f32_16x16x32_bf16 v[224:227], v[120:123], v[80:83], v[224:227]
ds_read_b64_tr_b16 v[48:49], v16 offset:16384
v_mfma_f32_16x16x32_bf16 v[240:243], v[124:127], v[80:83], v[240:243]
buffer_load_dwordx4 v11, s[20:23], s49 offen offset:1024 lds
v_mfma_f32_16x16x32_bf16 v[244:247], v[124:127], v[84:87], v[244:247]
ds_read_b64_tr_b16 v[50:51], v16 offset:16896
v_mfma_f32_16x16x32_bf16 v[228:231], v[120:123], v[84:87], v[228:231]
ds_read_b64_tr_b16 v[52:53], v17 offset:16384
v_mfma_f32_16x16x32_bf16 v[212:215], v[116:119], v[84:87], v[212:215]
ds_read_b64_tr_b16 v[54:55], v17 offset:16896
v_mfma_f32_16x16x32_bf16 v[196:199], v[112:115], v[84:87], v[196:199]
buffer_load_dwordx4 v12, s[20:23], s49 offen offset:2048 lds
v_mfma_f32_16x16x32_bf16 v[180:183], v[108:111], v[84:87], v[180:183]
ds_read_b64_tr_b16 v[56:57], v18 offset:16384
v_mfma_f32_16x16x32_bf16 v[164:167], v[104:107], v[84:87], v[164:167]
ds_read_b64_tr_b16 v[58:59], v18 offset:16896
v_mfma_f32_16x16x32_bf16 v[148:151], v[100:103], v[84:87], v[148:151]
ds_read_b64_tr_b16 v[60:61], v19 offset:16384
v_mfma_f32_16x16x32_bf16 v[132:135], v[96:99], v[84:87], v[132:135]
buffer_load_dwordx4 v13, s[20:23], s49 offen offset:3072 lds
v_mfma_f32_16x16x32_bf16 v[136:139], v[96:99], v[88:91], v[136:139]
ds_read_b64_tr_b16 v[62:63], v19 offset:16896
s_add_u32 s49, s49, 229376
v_mfma_f32_16x16x32_bf16 v[152:155], v[100:103], v[88:91], v[152:155]
ds_read_b64_tr_b16 v[64:65], v16 offset:24576
v_mfma_f32_16x16x32_bf16 v[168:171], v[104:107], v[88:91], v[168:171]
ds_read_b64_tr_b16 v[66:67], v16 offset:25088
s_add_u32 m0, s45, -768
v_mfma_f32_16x16x32_bf16 v[184:187], v[108:111], v[88:91], v[184:187]
buffer_load_dwordx4 v2, s[16:19], s47 offen offset:768 lds
v_mfma_f32_16x16x32_bf16 v[200:203], v[112:115], v[88:91], v[200:203]
ds_read_b64_tr_b16 v[68:69], v17 offset:24576
v_mfma_f32_16x16x32_bf16 v[216:219], v[116:119], v[88:91], v[216:219]
ds_read_b64_tr_b16 v[70:71], v17 offset:25088
v_mfma_f32_16x16x32_bf16 v[232:235], v[120:123], v[88:91], v[232:235]
ds_read_b64_tr_b16 v[72:73], v18 offset:24576
v_mfma_f32_16x16x32_bf16 v[248:251], v[124:127], v[88:91], v[248:251]
buffer_load_dwordx4 v3, s[16:19], s47 offen offset:1792 lds
v_mfma_f32_16x16x32_bf16 v[252:255], v[124:127], v[92:95], v[252:255]
ds_read_b64_tr_b16 v[74:75], v18 offset:25088
v_mfma_f32_16x16x32_bf16 v[236:239], v[120:123], v[92:95], v[236:239]
ds_read_b64_tr_b16 v[76:77], v19 offset:24576
v_mfma_f32_16x16x32_bf16 v[220:223], v[116:119], v[92:95], v[220:223]
ds_read_b64_tr_b16 v[78:79], v19 offset:25088
v_mfma_f32_16x16x32_bf16 v[204:207], v[112:115], v[92:95], v[204:207]
buffer_load_dwordx4 v4, s[16:19], s47 offen offset:2816 lds
v_mfma_f32_16x16x32_bf16 v[188:191], v[108:111], v[92:95], v[188:191]
v_mfma_f32_16x16x32_bf16 v[172:175], v[104:107], v[92:95], v[172:175]
v_mfma_f32_16x16x32_bf16 v[156:159], v[100:103], v[92:95], v[156:159]
v_mfma_f32_16x16x32_bf16 v[140:143], v[96:99], v[92:95], v[140:143]
buffer_load_dwordx4 v5, s[16:19], s47 offen offset:3840 lds
s_waitcnt lgkmcnt(0)
v_mfma_f32_16x16x32_bf16 v[128:131], v[48:51], v[32:35], v[128:131]
ds_read_b128 v[80:83], v15 offset:8192
v_mfma_f32_16x16x32_bf16 v[144:147], v[52:55], v[32:35], v[144:147]
ds_read_b128 v[84:87], v15 offset:10240
v_mfma_f32_16x16x32_bf16 v[160:163], v[56:59], v[32:35], v[160:163]
ds_read_b128 v[88:91], v15 offset:12288
s_add_u32 m0, s45, 3328
v_mfma_f32_16x16x32_bf16 v[176:179], v[60:63], v[32:35], v[176:179]
buffer_load_dwordx4 v2, s[16:19], s34 offen offset:768 lds
v_mfma_f32_16x16x32_bf16 v[192:195], v[64:67], v[32:35], v[192:195]
ds_read_b128 v[92:95], v15 offset:14336
v_mfma_f32_16x16x32_bf16 v[208:211], v[68:71], v[32:35], v[208:211]
ds_read_b64_tr_b16 v[96:97], v16 offset:20480
v_mfma_f32_16x16x32_bf16 v[224:227], v[72:75], v[32:35], v[224:227]
ds_read_b64_tr_b16 v[98:99], v16 offset:20992
v_mfma_f32_16x16x32_bf16 v[240:243], v[76:79], v[32:35], v[240:243]
buffer_load_dwordx4 v3, s[16:19], s34 offen offset:1792 lds
v_mfma_f32_16x16x32_bf16 v[244:247], v[76:79], v[36:39], v[244:247]
ds_read_b64_tr_b16 v[100:101], v17 offset:20480
v_mfma_f32_16x16x32_bf16 v[228:231], v[72:75], v[36:39], v[228:231]
ds_read_b64_tr_b16 v[102:103], v17 offset:20992
v_mfma_f32_16x16x32_bf16 v[212:215], v[68:71], v[36:39], v[212:215]
ds_read_b64_tr_b16 v[104:105], v18 offset:20480
v_mfma_f32_16x16x32_bf16 v[196:199], v[64:67], v[36:39], v[196:199]
buffer_load_dwordx4 v4, s[16:19], s34 offen offset:2816 lds
v_mfma_f32_16x16x32_bf16 v[180:183], v[60:63], v[36:39], v[180:183]
ds_read_b64_tr_b16 v[106:107], v18 offset:20992
v_mfma_f32_16x16x32_bf16 v[164:167], v[56:59], v[36:39], v[164:167]
ds_read_b64_tr_b16 v[108:109], v19 offset:20480
v_mfma_f32_16x16x32_bf16 v[148:151], v[52:55], v[36:39], v[148:151]
ds_read_b64_tr_b16 v[110:111], v19 offset:20992
v_mfma_f32_16x16x32_bf16 v[132:135], v[48:51], v[36:39], v[132:135]
buffer_load_dwordx4 v5, s[16:19], s34 offen offset:3840 lds
v_mfma_f32_16x16x32_bf16 v[136:139], v[48:51], v[40:43], v[136:139]
ds_read_b64_tr_b16 v[112:113], v16 offset:28672
v_mfma_f32_16x16x32_bf16 v[152:155], v[52:55], v[40:43], v[152:155]
ds_read_b64_tr_b16 v[114:115], v16 offset:29184
v_mfma_f32_16x16x32_bf16 v[168:171], v[56:59], v[40:43], v[168:171]
ds_read_b64_tr_b16 v[116:117], v17 offset:28672
v_mfma_f32_16x16x32_bf16 v[184:187], v[60:63], v[40:43], v[184:187]
v_mfma_f32_16x16x32_bf16 v[200:203], v[64:67], v[40:43], v[200:203]
ds_read_b64_tr_b16 v[118:119], v17 offset:29184
v_mfma_f32_16x16x32_bf16 v[216:219], v[68:71], v[40:43], v[216:219]
ds_read_b64_tr_b16 v[120:121], v18 offset:28672
v_mfma_f32_16x16x32_bf16 v[232:235], v[72:75], v[40:43], v[232:235]
ds_read_b64_tr_b16 v[122:123], v18 offset:29184
v_mfma_f32_16x16x32_bf16 v[248:251], v[76:79], v[40:43], v[248:251]
v_mfma_f32_16x16x32_bf16 v[252:255], v[76:79], v[44:47], v[252:255]
ds_read_b64_tr_b16 v[124:125], v19 offset:28672
v_mfma_f32_16x16x32_bf16 v[236:239], v[72:75], v[44:47], v[236:239]
ds_read_b64_tr_b16 v[126:127], v19 offset:29184
v_mfma_f32_16x16x32_bf16 v[220:223], v[68:71], v[44:47], v[220:223]
v_mfma_f32_16x16x32_bf16 v[204:207], v[64:67], v[44:47], v[204:207]
v_mfma_f32_16x16x32_bf16 v[188:191], v[60:63], v[44:47], v[188:191]
buffer_load_dword v9, v6, s[36:39], s57 offen sc1
v_mfma_f32_16x16x32_bf16 v[172:175], v[56:59], v[44:47], v[172:175]
v_mfma_f32_16x16x32_bf16 v[156:159], v[52:55], v[44:47], v[156:159]
v_mfma_f32_16x16x32_bf16 v[140:143], v[48:51], v[44:47], v[140:143]
s_waitcnt lgkmcnt(0)
v_mfma_f32_16x16x32_bf16 v[128:131], v[96:99], v[80:83], v[128:131]
s_waitcnt vmcnt(13)
s_barrier
v_mfma_f32_16x16x32_bf16 v[144:147], v[100:103], v[80:83], v[144:147]
ds_read_b128 v[32:35], v14 offset:16384
v_mfma_f32_16x16x32_bf16 v[160:163], v[104:107], v[80:83], v[160:163]
ds_read_b128 v[36:39], v14 offset:18432
s_add_u32 m0, s46, 16384
v_mfma_f32_16x16x32_bf16 v[176:179], v[108:111], v[80:83], v[176:179]
buffer_load_dwordx4 v10, s[20:23], s49 offen offset:0 lds
v_mfma_f32_16x16x32_bf16 v[192:195], v[112:115], v[80:83], v[192:195]
ds_read_b128 v[40:43], v14 offset:20480
v_mfma_f32_16x16x32_bf16 v[208:211], v[116:119], v[80:83], v[208:211]
ds_read_b128 v[44:47], v14 offset:22528
v_mfma_f32_16x16x32_bf16 v[224:227], v[120:123], v[80:83], v[224:227]
ds_read_b64_tr_b16 v[48:49], v16 offset:32768
v_mfma_f32_16x16x32_bf16 v[240:243], v[124:127], v[80:83], v[240:243]
buffer_load_dwordx4 v11, s[20:23], s49 offen offset:1024 lds
v_mfma_f32_16x16x32_bf16 v[244:247], v[124:127], v[84:87], v[244:247]
ds_read_b64_tr_b16 v[50:51], v16 offset:33280
v_mfma_f32_16x16x32_bf16 v[228:231], v[120:123], v[84:87], v[228:231]
ds_read_b64_tr_b16 v[52:53], v17 offset:32768
v_mfma_f32_16x16x32_bf16 v[212:215], v[116:119], v[84:87], v[212:215]
ds_read_b64_tr_b16 v[54:55], v17 offset:33280
v_mfma_f32_16x16x32_bf16 v[196:199], v[112:115], v[84:87], v[196:199]
buffer_load_dwordx4 v12, s[20:23], s49 offen offset:2048 lds
v_mfma_f32_16x16x32_bf16 v[180:183], v[108:111], v[84:87], v[180:183]
ds_read_b64_tr_b16 v[56:57], v18 offset:32768
v_mfma_f32_16x16x32_bf16 v[164:167], v[104:107], v[84:87], v[164:167]
ds_read_b64_tr_b16 v[58:59], v18 offset:33280
v_mfma_f32_16x16x32_bf16 v[148:151], v[100:103], v[84:87], v[148:151]
ds_read_b64_tr_b16 v[60:61], v19 offset:32768
v_mfma_f32_16x16x32_bf16 v[132:135], v[96:99], v[84:87], v[132:135]
buffer_load_dwordx4 v13, s[20:23], s49 offen offset:3072 lds
v_mfma_f32_16x16x32_bf16 v[136:139], v[96:99], v[88:91], v[136:139]
ds_read_b64_tr_b16 v[62:63], v19 offset:33280
v_mfma_f32_16x16x32_bf16 v[152:155], v[100:103], v[88:91], v[152:155]
ds_read_b64_tr_b16 v[64:65], v16 offset:40960
v_mfma_f32_16x16x32_bf16 v[168:171], v[104:107], v[88:91], v[168:171]
ds_read_b64_tr_b16 v[66:67], v16 offset:41472
s_add_u32 m0, s45, 7296
v_mfma_f32_16x16x32_bf16 v[184:187], v[108:111], v[88:91], v[184:187]
buffer_load_dwordx4 v2, s[16:19], s47 offen offset:896 lds
v_mfma_f32_16x16x32_bf16 v[200:203], v[112:115], v[88:91], v[200:203]
ds_read_b64_tr_b16 v[68:69], v17 offset:40960
v_mfma_f32_16x16x32_bf16 v[216:219], v[116:119], v[88:91], v[216:219]
ds_read_b64_tr_b16 v[70:71], v17 offset:41472
v_mfma_f32_16x16x32_bf16 v[232:235], v[120:123], v[88:91], v[232:235]
ds_read_b64_tr_b16 v[72:73], v18 offset:40960
v_mfma_f32_16x16x32_bf16 v[248:251], v[124:127], v[88:91], v[248:251]
buffer_load_dwordx4 v3, s[16:19], s47 offen offset:1920 lds
v_mfma_f32_16x16x32_bf16 v[252:255], v[124:127], v[92:95], v[252:255]
ds_read_b64_tr_b16 v[74:75], v18 offset:41472
v_mfma_f32_16x16x32_bf16 v[236:239], v[120:123], v[92:95], v[236:239]
ds_read_b64_tr_b16 v[76:77], v19 offset:40960
v_mfma_f32_16x16x32_bf16 v[220:223], v[116:119], v[92:95], v[220:223]
ds_read_b64_tr_b16 v[78:79], v19 offset:41472
v_mfma_f32_16x16x32_bf16 v[204:207], v[112:115], v[92:95], v[204:207]
buffer_load_dwordx4 v4, s[16:19], s47 offen offset:2944 lds
v_mfma_f32_16x16x32_bf16 v[188:191], v[108:111], v[92:95], v[188:191]
v_mfma_f32_16x16x32_bf16 v[172:175], v[104:107], v[92:95], v[172:175]
v_mfma_f32_16x16x32_bf16 v[156:159], v[100:103], v[92:95], v[156:159]
v_mfma_f32_16x16x32_bf16 v[140:143], v[96:99], v[92:95], v[140:143]
buffer_load_dwordx4 v5, s[16:19], s47 offen offset:3968 lds
s_waitcnt lgkmcnt(0)
v_mfma_f32_16x16x32_bf16 v[128:131], v[48:51], v[32:35], v[128:131]
ds_read_b128 v[80:83], v15 offset:16384
v_mfma_f32_16x16x32_bf16 v[144:147], v[52:55], v[32:35], v[144:147]
ds_read_b128 v[84:87], v15 offset:18432
v_mfma_f32_16x16x32_bf16 v[160:163], v[56:59], v[32:35], v[160:163]
ds_read_b128 v[88:91], v15 offset:20480
s_add_u32 m0, s45, 11392
v_mfma_f32_16x16x32_bf16 v[176:179], v[60:63], v[32:35], v[176:179]
buffer_load_dwordx4 v2, s[16:19], s34 offen offset:896 lds
v_mfma_f32_16x16x32_bf16 v[192:195], v[64:67], v[32:35], v[192:195]
ds_read_b128 v[92:95], v15 offset:22528
v_mfma_f32_16x16x32_bf16 v[208:211], v[68:71], v[32:35], v[208:211]
ds_read_b64_tr_b16 v[96:97], v16 offset:36864
v_mfma_f32_16x16x32_bf16 v[224:227], v[72:75], v[32:35], v[224:227]
ds_read_b64_tr_b16 v[98:99], v16 offset:37376
v_mfma_f32_16x16x32_bf16 v[240:243], v[76:79], v[32:35], v[240:243]
buffer_load_dwordx4 v3, s[16:19], s34 offen offset:1920 lds
v_mfma_f32_16x16x32_bf16 v[244:247], v[76:79], v[36:39], v[244:247]
ds_read_b64_tr_b16 v[100:101], v17 offset:36864
v_mfma_f32_16x16x32_bf16 v[228:231], v[72:75], v[36:39], v[228:231]
ds_read_b64_tr_b16 v[102:103], v17 offset:37376
v_mfma_f32_16x16x32_bf16 v[212:215], v[68:71], v[36:39], v[212:215]
ds_read_b64_tr_b16 v[104:105], v18 offset:36864
v_mfma_f32_16x16x32_bf16 v[196:199], v[64:67], v[36:39], v[196:199]
buffer_load_dwordx4 v4, s[16:19], s34 offen offset:2944 lds
v_mfma_f32_16x16x32_bf16 v[180:183], v[60:63], v[36:39], v[180:183]
ds_read_b64_tr_b16 v[106:107], v18 offset:37376
v_mfma_f32_16x16x32_bf16 v[164:167], v[56:59], v[36:39], v[164:167]
ds_read_b64_tr_b16 v[108:109], v19 offset:36864
v_mfma_f32_16x16x32_bf16 v[148:151], v[52:55], v[36:39], v[148:151]
ds_read_b64_tr_b16 v[110:111], v19 offset:37376
v_mfma_f32_16x16x32_bf16 v[132:135], v[48:51], v[36:39], v[132:135]
buffer_load_dwordx4 v5, s[16:19], s34 offen offset:3968 lds
v_mfma_f32_16x16x32_bf16 v[136:139], v[48:51], v[40:43], v[136:139]
ds_read_b64_tr_b16 v[112:113], v16 offset:45056
v_mfma_f32_16x16x32_bf16 v[152:155], v[52:55], v[40:43], v[152:155]
ds_read_b64_tr_b16 v[114:115], v16 offset:45568
v_mfma_f32_16x16x32_bf16 v[168:171], v[56:59], v[40:43], v[168:171]
ds_read_b64_tr_b16 v[116:117], v17 offset:45056
s_add_u32 m0, s45, 24576
s_lshl3_add_u32 s63, s4, s51
v_mfma_f32_16x16x32_bf16 v[184:187], v[60:63], v[40:43], v[184:187]
buffer_load_dwordx4 v20, s[24:27], s63 offen offset:0 lds
v_mfma_f32_16x16x32_bf16 v[200:203], v[64:67], v[40:43], v[200:203]
ds_read_b64_tr_b16 v[118:119], v17 offset:45568
v_mfma_f32_16x16x32_bf16 v[216:219], v[68:71], v[40:43], v[216:219]
ds_read_b64_tr_b16 v[120:121], v18 offset:45056
v_mfma_f32_16x16x32_bf16 v[232:235], v[72:75], v[40:43], v[232:235]
ds_read_b64_tr_b16 v[122:123], v18 offset:45568
v_mfma_f32_16x16x32_bf16 v[248:251], v[76:79], v[40:43], v[248:251]
buffer_load_dwordx4 v21, s[24:27], s63 offen offset:1024 lds
v_mfma_f32_16x16x32_bf16 v[252:255], v[76:79], v[44:47], v[252:255]
ds_read_b64_tr_b16 v[124:125], v19 offset:45056
v_mfma_f32_16x16x32_bf16 v[236:239], v[72:75], v[44:47], v[236:239]
ds_read_b64_tr_b16 v[126:127], v19 offset:45568
v_mfma_f32_16x16x32_bf16 v[220:223], v[68:71], v[44:47], v[220:223]
v_mfma_f32_16x16x32_bf16 v[204:207], v[64:67], v[44:47], v[204:207]
buffer_load_dwordx4 v22, s[24:27], s63 offen offset:2048 lds
v_mfma_f32_16x16x32_bf16 v[188:191], v[60:63], v[44:47], v[188:191]
s_waitcnt vmcnt(15)
v_readfirstlane_b32 s60, v9
v_mov_b32 v9, 1
s_or_b32 s60, s60, s44
s_cbranch_scc0 .L_ackslow_p_%=
.L_ackok_p_%=:
buffer_store_dword v9, v6, s[36:39], s57 offen offset:64 sc1
v_mfma_f32_16x16x32_bf16 v[172:175], v[56:59], v[44:47], v[172:175]
v_mfma_f32_16x16x32_bf16 v[156:159], v[52:55], v[44:47], v[156:159]
v_mfma_f32_16x16x32_bf16 v[140:143], v[48:51], v[44:47], v[140:143]
buffer_load_dwordx4 v23, s[24:27], s63 offen offset:3072 lds
s_waitcnt lgkmcnt(0)
v_mfma_f32_16x16x32_bf16 v[128:131], v[96:99], v[80:83], v[128:131]
s_barrier
v_mfma_f32_16x16x32_bf16 v[144:147], v[100:103], v[80:83], v[144:147]
ds_read_b128 v[32:35], v14 offset:0
v_mfma_f32_16x16x32_bf16 v[160:163], v[104:107], v[80:83], v[160:163]
ds_read_b128 v[36:39], v14 offset:2048
s_add_u32 m0, s45, 20480
s_lshl3_add_u32 s63, s4, s58
v_mfma_f32_16x16x32_bf16 v[176:179], v[108:111], v[80:83], v[176:179]
buffer_load_dwordx4 v20, s[24:27], s63 offen offset:0 lds
v_mfma_f32_16x16x32_bf16 v[192:195], v[112:115], v[80:83], v[192:195]
ds_read_b128 v[40:43], v14 offset:4096
v_mfma_f32_16x16x32_bf16 v[208:211], v[116:119], v[80:83], v[208:211]
ds_read_b128 v[44:47], v14 offset:6144
v_mfma_f32_16x16x32_bf16 v[224:227], v[120:123], v[80:83], v[224:227]
ds_read_b64_tr_b16 v[48:49], v16 offset:0
v_mfma_f32_16x16x32_bf16 v[240:243], v[124:127], v[80:83], v[240:243]
buffer_load_dwordx4 v21, s[24:27], s63 offen offset:1024 lds
v_mfma_f32_16x16x32_bf16 v[244:247], v[124:127], v[84:87], v[244:247]
ds_read_b64_tr_b16 v[50:51], v16 offset:512
v_mfma_f32_16x16x32_bf16 v[228:231], v[120:123], v[84:87], v[228:231]
ds_read_b64_tr_b16 v[52:53], v16 offset:8192
v_mfma_f32_16x16x32_bf16 v[212:215], v[116:119], v[84:87], v[212:215]
ds_read_b64_tr_b16 v[54:55], v16 offset:8704
v_mfma_f32_16x16x32_bf16 v[196:199], v[112:115], v[84:87], v[196:199]
buffer_load_dwordx4 v22, s[24:27], s63 offen offset:2048 lds
v_mfma_f32_16x16x32_bf16 v[180:183], v[108:111], v[84:87], v[180:183]
ds_read_b64_tr_b16 v[56:57], v17 offset:0
v_mfma_f32_16x16x32_bf16 v[164:167], v[104:107], v[84:87], v[164:167]
ds_read_b64_tr_b16 v[58:59], v17 offset:512
v_mfma_f32_16x16x32_bf16 v[148:151], v[100:103], v[84:87], v[148:151]
ds_read_b64_tr_b16 v[60:61], v17 offset:8192
v_mfma_f32_16x16x32_bf16 v[132:135], v[96:99], v[84:87], v[132:135]
buffer_load_dwordx4 v23, s[24:27], s63 offen offset:3072 lds
v_mfma_f32_16x16x32_bf16 v[136:139], v[96:99], v[88:91], v[136:139]
ds_read_b64_tr_b16 v[62:63], v17 offset:8704
v_mfma_f32_16x16x32_bf16 v[152:155], v[100:103], v[88:91], v[152:155]
ds_read_b128 v[64:67], v15 offset:0
v_mfma_f32_16x16x32_bf16 v[168:171], v[104:107], v[88:91], v[168:171]
ds_read_b128 v[68:71], v15 offset:2048
s_add_u32 m0, s45, 16384
s_lshl3_add_u32 s63, s5, s51
v_mfma_f32_16x16x32_bf16 v[184:187], v[108:111], v[88:91], v[184:187]
buffer_load_dwordx4 v20, s[24:27], s63 offen offset:0 lds
v_mfma_f32_16x16x32_bf16 v[200:203], v[112:115], v[88:91], v[200:203]
ds_read_b128 v[72:75], v15 offset:4096
v_mfma_f32_16x16x32_bf16 v[216:219], v[116:119], v[88:91], v[216:219]
ds_read_b128 v[76:79], v15 offset:6144
v_mfma_f32_16x16x32_bf16 v[232:235], v[120:123], v[88:91], v[232:235]
ds_read_b64_tr_b16 a[240:241], v18 offset:4096
v_mfma_f32_16x16x32_bf16 v[248:251], v[124:127], v[88:91], v[248:251]
buffer_load_dwordx4 v21, s[24:27], s63 offen offset:1024 lds
v_mfma_f32_16x16x32_bf16 v[252:255], v[124:127], v[92:95], v[252:255]
ds_read_b64_tr_b16 a[242:243], v18 offset:4608
v_mfma_f32_16x16x32_bf16 v[236:239], v[120:123], v[92:95], v[236:239]
ds_read_b64_tr_b16 a[244:245], v18 offset:12288
v_mfma_f32_16x16x32_bf16 v[220:223], v[116:119], v[92:95], v[220:223]
ds_read_b64_tr_b16 a[246:247], v18 offset:12800
v_mfma_f32_16x16x32_bf16 v[204:207], v[112:115], v[92:95], v[204:207]
buffer_load_dwordx4 v22, s[24:27], s63 offen offset:2048 lds
v_mfma_f32_16x16x32_bf16 v[188:191], v[108:111], v[92:95], v[188:191]
v_mfma_f32_16x16x32_bf16 v[172:175], v[104:107], v[92:95], v[172:175]
v_mfma_f32_16x16x32_bf16 v[156:159], v[100:103], v[92:95], v[156:159]
v_mfma_f32_16x16x32_bf16 v[140:143], v[96:99], v[92:95], v[140:143]
buffer_load_dwordx4 v23, s[24:27], s63 offen offset:3072 lds
s_waitcnt lgkmcnt(4)
v_mfma_f32_16x16x32_bf16 v[128:131], v[48:51], v[32:35], v[128:131]
ds_read_b64_tr_b16 v[96:97], v16 offset:4096
v_mfma_f32_16x16x32_bf16 v[192:195], v[52:55], v[32:35], v[192:195]
ds_read_b64_tr_b16 v[98:99], v16 offset:4608
v_mfma_f32_16x16x32_bf16 v[144:147], v[56:59], v[32:35], v[144:147]
ds_read_b64_tr_b16 v[100:101], v16 offset:12288
s_add_u32 m0, s45, 4096
s_lshl3_add_u32 s63, s5, s58
v_mfma_f32_16x16x32_bf16 v[208:211], v[60:63], v[32:35], v[208:211]
buffer_load_dwordx4 v20, s[24:27], s63 offen offset:0 lds
v_mfma_f32_16x16x32_bf16 v[212:215], v[60:63], v[36:39], v[212:215]
ds_read_b64_tr_b16 v[102:103], v16 offset:12800
v_mfma_f32_16x16x32_bf16 v[148:151], v[56:59], v[36:39], v[148:151]
ds_read_b64_tr_b16 v[104:105], v17 offset:4096
v_mfma_f32_16x16x32_bf16 v[196:199], v[52:55], v[36:39], v[196:199]
ds_read_b64_tr_b16 v[106:107], v17 offset:4608
v_mfma_f32_16x16x32_bf16 v[132:135], v[48:51], v[36:39], v[132:135]
buffer_load_dwordx4 v21, s[24:27], s63 offen offset:1024 lds
v_mfma_f32_16x16x32_bf16 v[136:139], v[48:51], v[40:43], v[136:139]
ds_read_b64_tr_b16 v[108:109], v17 offset:12288
v_mfma_f32_16x16x32_bf16 v[200:203], v[52:55], v[40:43], v[200:203]
ds_read_b64_tr_b16 v[110:111], v17 offset:12800
v_mfma_f32_16x16x32_bf16 v[152:155], v[56:59], v[40:43], v[152:155]
ds_read_b64_tr_b16 a[248:249], v19 offset:4096
s_waitcnt vmcnt(10)
v_mfma_f32_16x16x32_bf16 v[216:219], v[60:63], v[40:43], v[216:219]
buffer_load_dwordx4 v22, s[24:27], s63 offen offset:2048 lds
v_mfma_f32_16x16x32_bf16 v[220:223], v[60:63], v[44:47], v[220:223]
ds_read_b64_tr_b16 a[250:251], v19 offset:4608
ds_read_b64_tr_b16 a[0:1], v24 offset:24576
v_mfma_f32_16x16x32_bf16 v[156:159], v[56:59], v[44:47], v[156:159]
ds_read_b64_tr_b16 a[252:253], v19 offset:12288
ds_read_b64_tr_b16 a[2:3], v24 offset:26624
v_mfma_f32_16x16x32_bf16 v[204:207], v[52:55], v[44:47], v[204:207]
ds_read_b64_tr_b16 a[254:255], v19 offset:12800
ds_read_b64_tr_b16 a[4:5], v25 offset:24576
v_mfma_f32_16x16x32_bf16 v[140:143], v[48:51], v[44:47], v[140:143]
buffer_load_dwordx4 v23, s[24:27], s63 offen offset:3072 lds
s_waitcnt lgkmcnt(7)
v_mfma_f32_16x16x32_bf16 v[128:131], v[96:99], v[64:67], v[128:131]
ds_read_b64_tr_b16 a[6:7], v25 offset:26624
ds_read_b64_tr_b16 a[8:9], v26 offset:24576
s_barrier
v_mfma_f32_16x16x32_bf16 v[192:195], v[100:103], v[64:67], v[192:195]
ds_read_b128 v[80:83], v14 offset:8192
ds_read_b128 v[84:87], v14 offset:10240
v_mfma_f32_16x16x32_bf16 v[144:147], v[104:107], v[64:67], v[144:147]
ds_read_b128 v[88:91], v14 offset:12288
ds_read_b128 v[92:95], v14 offset:14336
s_add_u32 m0, s45, 0
s_lshl3_add_u32 s63, s6, s51
v_mfma_f32_16x16x32_bf16 v[208:211], v[108:111], v[64:67], v[208:211]
buffer_load_dwordx4 v20, s[24:27], s63 offen offset:0 lds
v_mfma_f32_16x16x32_bf16 v[212:215], v[108:111], v[68:71], v[212:215]
ds_read_b64_tr_b16 v[48:49], v16 offset:16384
ds_read_b64_tr_b16 v[50:51], v16 offset:16896
v_mfma_f32_16x16x32_bf16 v[148:151], v[104:107], v[68:71], v[148:151]
ds_read_b64_tr_b16 v[52:53], v16 offset:24576
ds_read_b64_tr_b16 v[54:55], v16 offset:25088
v_mfma_f32_16x16x32_bf16 v[196:199], v[100:103], v[68:71], v[196:199]
ds_read_b64_tr_b16 v[56:57], v17 offset:16384
ds_read_b64_tr_b16 v[58:59], v17 offset:16896
v_mfma_f32_16x16x32_bf16 v[132:135], v[96:99], v[68:71], v[132:135]
buffer_load_dwordx4 v21, s[24:27], s63 offen offset:1024 lds
v_mfma_f32_16x16x32_bf16 v[136:139], v[96:99], v[72:75], v[136:139]
ds_read_b64_tr_b16 v[60:61], v17 offset:24576
ds_read_b64_tr_b16 v[62:63], v17 offset:25088
v_mfma_f32_16x16x32_bf16 v[200:203], v[100:103], v[72:75], v[200:203]
ds_read_b64_tr_b16 a[10:11], v26 offset:26624
ds_read_b64_tr_b16 a[12:13], v27 offset:24576
v_mfma_f32_16x16x32_bf16 v[152:155], v[104:107], v[72:75], v[152:155]
ds_read_b64_tr_b16 a[224:225], v18 offset:0
ds_read_b64_tr_b16 a[14:15], v27 offset:26624
s_waitcnt vmcnt(10)
v_mfma_f32_16x16x32_bf16 v[216:219], v[108:111], v[72:75], v[216:219]
buffer_load_dwordx4 v22, s[24:27], s63 offen offset:2048 lds
v_mfma_f32_16x16x32_bf16 v[220:223], v[108:111], v[76:79], v[220:223]
ds_read_b64_tr_b16 a[226:227], v18 offset:512
ds_read_b64_tr_b16 a[112:113], v24 offset:20480
v_mfma_f32_16x16x32_bf16 v[156:159], v[104:107], v[76:79], v[156:159]
ds_read_b64_tr_b16 a[228:229], v18 offset:8192
ds_read_b64_tr_b16 a[114:115], v24 offset:22528
v_mfma_f32_16x16x32_bf16 v[204:207], v[100:103], v[76:79], v[204:207]
ds_read_b64_tr_b16 a[230:231], v18 offset:8704
ds_read_b64_tr_b16 a[116:117], v25 offset:20480
v_mfma_f32_16x16x32_bf16 v[140:143], v[96:99], v[76:79], v[140:143]
buffer_load_dwordx4 v23, s[24:27], s63 offen offset:3072 lds
s_waitcnt lgkmcnt(10)
v_mfma_f32_16x16x32_bf16 v[128:131], v[48:51], v[80:83], v[128:131]
ds_read_b128 v[112:115], v15 offset:8192
ds_read_b128 v[116:119], v15 offset:10240
v_mfma_f32_16x16x32_bf16 v[192:195], v[52:55], v[80:83], v[192:195]
ds_read_b128 v[120:123], v15 offset:12288
ds_read_b128 v[124:127], v15 offset:14336
v_mfma_f32_16x16x32_bf16 v[144:147], v[56:59], v[80:83], v[144:147]
ds_read_b64_tr_b16 v[96:97], v16 offset:20480
ds_read_b64_tr_b16 v[98:99], v16 offset:20992
s_waitcnt lgkmcnt(12)
s_add_u32 m0, s45, 24576
s_lshl3_add_u32 s63, s6, s58
v_mfma_f32_16x16x32_bf16 v[208:211], v[60:63], v[80:83], v[208:211]
buffer_load_dwordx4 v20, s[24:27], s63 offen offset:0 lds
v_mfma_f32_16x16x32_bf16 v[212:215], v[60:63], v[84:87], v[212:215]
ds_read_b64_tr_b16 v[100:101], v16 offset:28672
ds_read_b64_tr_b16 v[102:103], v16 offset:29184
v_mfma_f32_16x16x32_bf16 v[148:151], v[56:59], v[84:87], v[148:151]
ds_read_b64_tr_b16 v[104:105], v17 offset:20480
ds_read_b64_tr_b16 v[106:107], v17 offset:20992
v_mfma_f32_16x16x32_bf16 v[196:199], v[52:55], v[84:87], v[196:199]
ds_read_b64_tr_b16 v[108:109], v17 offset:28672
ds_read_b64_tr_b16 v[110:111], v17 offset:29184
v_mfma_f32_16x16x32_bf16 v[132:135], v[48:51], v[84:87], v[132:135]
buffer_load_dwordx4 v21, s[24:27], s63 offen offset:1024 lds
v_mfma_f32_16x16x32_bf16 v[136:139], v[48:51], v[88:91], v[136:139]
ds_read_b64_tr_b16 a[118:119], v25 offset:22528
ds_read_b64_tr_b16 a[120:121], v26 offset:20480
v_mfma_f32_16x16x32_bf16 v[200:203], v[52:55], v[88:91], v[200:203]
ds_read_b64_tr_b16 a[232:233], v19 offset:0
ds_read_b64_tr_b16 a[122:123], v26 offset:22528
v_mfma_f32_16x16x32_bf16 v[152:155], v[56:59], v[88:91], v[152:155]
ds_read_b64_tr_b16 a[234:235], v19 offset:512
ds_read_b64_tr_b16 a[124:125], v27 offset:20480
v_mfma_f32_16x16x32_bf16 v[216:219], v[60:63], v[88:91], v[216:219]
buffer_load_dwordx4 v22, s[24:27], s63 offen offset:2048 lds
v_mfma_f32_16x16x32_bf16 v[220:223], v[60:63], v[92:95], v[220:223]
ds_read_b64_tr_b16 a[236:237], v19 offset:8192
ds_read_b64_tr_b16 a[126:127], v27 offset:22528
v_mfma_f32_16x16x32_bf16 v[156:159], v[56:59], v[92:95], v[156:159]
ds_read_b64_tr_b16 a[238:239], v19 offset:8704
v_mfma_f32_16x16x32_bf16 v[204:207], v[52:55], v[92:95], v[204:207]
v_mfma_f32_16x16x32_bf16 v[140:143], v[48:51], v[92:95], v[140:143]
buffer_load_dwordx4 v23, s[24:27], s63 offen offset:3072 lds
s_waitcnt lgkmcnt(9)
v_mfma_f32_16x16x32_bf16 v[128:131], v[96:99], v[112:115], v[128:131]
ds_read_b64_tr_b16 v[48:49], v18 offset:16384
ds_read_b64_tr_b16 v[50:51], v18 offset:16896
v_mfma_f32_16x16x32_bf16 v[192:195], v[100:103], v[112:115], v[192:195]
ds_read_b64_tr_b16 v[52:53], v18 offset:24576
ds_read_b64_tr_b16 v[54:55], v18 offset:25088
v_mfma_f32_16x16x32_bf16 v[144:147], v[104:107], v[112:115], v[144:147]
ds_read_b64_tr_b16 v[56:57], v19 offset:16384
ds_read_b64_tr_b16 v[58:59], v19 offset:16896
s_waitcnt lgkmcnt(7)
s_add_u32 m0, s45, 20480
s_lshl3_add_u32 s63, s7, s51
v_mfma_f32_16x16x32_bf16 v[208:211], v[108:111], v[112:115], v[208:211]
buffer_load_dwordx4 v20, s[24:27], s63 offen offset:0 lds
v_mfma_f32_16x16x32_bf16 v[212:215], v[108:111], v[116:119], v[212:215]
ds_read_b64_tr_b16 v[60:61], v19 offset:24576
ds_read_b64_tr_b16 v[62:63], v19 offset:25088
v_mfma_f32_16x16x32_bf16 v[148:151], v[104:107], v[116:119], v[148:151]
v_mfma_f32_16x16x32_bf16 v[196:199], v[100:103], v[116:119], v[196:199]
v_mfma_f32_16x16x32_bf16 v[132:135], v[96:99], v[116:119], v[132:135]
buffer_load_dwordx4 v21, s[24:27], s63 offen offset:1024 lds
v_mfma_f32_16x16x32_bf16 v[136:139], v[96:99], v[120:123], v[136:139]
v_mul_f32 v128, v128, v192
v_mul_f32 v129, v129, v193
v_mfma_f32_16x16x32_bf16 v[200:203], v[100:103], v[120:123], v[200:203]
v_mul_f32 v130, v130, v194
v_mul_f32 v131, v131, v195
v_mfma_f32_16x16x32_bf16 v[152:155], v[104:107], v[120:123], v[152:155]
v_mul_f32 v144, v144, v208
v_mul_f32 v145, v145, v209
v_mfma_f32_16x16x32_bf16 v[216:219], v[108:111], v[120:123], v[216:219]
buffer_load_dwordx4 v22, s[24:27], s63 offen offset:2048 lds
v_mfma_f32_16x16x32_bf16 v[220:223], v[108:111], v[124:127], v[220:223]
v_mul_f32 v146, v146, v210
v_mul_f32 v147, v147, v211
v_mfma_f32_16x16x32_bf16 v[156:159], v[104:107], v[124:127], v[156:159]
v_cvt_pk_bf16_f32 v192, v128, v129
v_cvt_pk_bf16_f32 v193, v130, v131
v_mfma_f32_16x16x32_bf16 v[204:207], v[100:103], v[124:127], v[204:207]
v_cvt_pk_bf16_f32 v194, v144, v145
v_cvt_pk_bf16_f32 v195, v146, v147
v_mfma_f32_16x16x32_bf16 v[140:143], v[96:99], v[124:127], v[140:143]
buffer_load_dwordx4 v23, s[24:27], s63 offen offset:3072 lds
s_waitcnt lgkmcnt(8)
v_mfma_f32_16x16x32_bf16 v[160:163], a[224:227], v[32:35], v[160:163]
ds_read_b64_tr_b16 v[96:97], v18 offset:20480
ds_read_b64_tr_b16 v[98:99], v18 offset:20992
v_mfma_f32_16x16x32_bf16 v[224:227], a[228:231], v[32:35], v[224:227]
ds_read_b64_tr_b16 v[100:101], v18 offset:28672
ds_read_b64_tr_b16 v[102:103], v18 offset:29184
v_mfma_f32_16x16x32_bf16 v[176:179], a[232:235], v[32:35], v[176:179]
ds_read_b64_tr_b16 v[104:105], v19 offset:20480
ds_read_b64_tr_b16 v[106:107], v19 offset:20992
v_mfma_f32_16x16x32_bf16 v[240:243], a[236:239], v[32:35], v[240:243]
v_mfma_f32_16x16x32_bf16 v[160:163], a[240:243], v[64:67], v[160:163]
ds_read_b64_tr_b16 v[108:109], v19 offset:28672
ds_read_b64_tr_b16 v[110:111], v19 offset:29184
v_mfma_f32_16x16x32_bf16 v[224:227], a[244:247], v[64:67], v[224:227]
buffer_store_dwordx4 v[192:195], v7, s[36:39], s52 offen offset:0 sc1
v_mul_f32 v132, v132, v196
v_mfma_f32_16x16x32_bf16 v[176:179], a[248:251], v[64:67], v[176:179]
v_mul_f32 v133, v133, v197
v_mul_f32 v134, v134, v198
v_mfma_f32_16x16x32_bf16 v[240:243], a[252:255], v[64:67], v[240:243]
s_waitcnt lgkmcnt(8)
v_mfma_f32_16x16x32_bf16 v[160:163], v[48:51], v[80:83], v[160:163]
v_mul_f32 v135, v135, v199
v_mul_f32 v148, v148, v212
v_mfma_f32_16x16x32_bf16 v[224:227], v[52:55], v[80:83], v[224:227]
v_mul_f32 v149, v149, v213
v_mul_f32 v150, v150, v214
v_mfma_f32_16x16x32_bf16 v[176:179], v[56:59], v[80:83], v[176:179]
v_mul_f32 v151, v151, v215
v_cvt_pk_bf16_f32 v196, v132, v133
v_mfma_f32_16x16x32_bf16 v[240:243], v[60:63], v[80:83], v[240:243]
s_waitcnt lgkmcnt(0)
v_mfma_f32_16x16x32_bf16 v[160:163], v[96:99], v[112:115], v[160:163]
v_cvt_pk_bf16_f32 v197, v134, v135
v_cvt_pk_bf16_f32 v198, v148, v149
v_mfma_f32_16x16x32_bf16 v[224:227], v[100:103], v[112:115], v[224:227]
v_cvt_pk_bf16_f32 v199, v150, v151
buffer_store_dwordx4 v[196:199], v7, s[36:39], s52 offen offset:2048 sc1
v_mfma_f32_16x16x32_bf16 v[176:179], v[104:107], v[112:115], v[176:179]
v_mul_f32 v136, v136, v200
v_mul_f32 v137, v137, v201
v_mfma_f32_16x16x32_bf16 v[240:243], v[108:111], v[112:115], v[240:243]
v_mfma_f32_16x16x32_bf16 v[164:167], a[224:227], v[36:39], v[164:167]
v_mul_f32 v138, v138, v202
v_mul_f32 v139, v139, v203
s_barrier
v_mfma_f32_16x16x32_bf16 v[228:231], a[228:231], v[36:39], v[228:231]
v_mul_f32 v152, v152, v216
v_mul_f32 v153, v153, v217
v_mfma_f32_16x16x32_bf16 v[180:183], a[232:235], v[36:39], v[180:183]
v_mul_f32 v154, v154, v218
v_mul_f32 v155, v155, v219
v_mfma_f32_16x16x32_bf16 v[244:247], a[236:239], v[36:39], v[244:247]
v_mfma_f32_16x16x32_bf16 v[164:167], a[240:243], v[68:71], v[164:167]
v_cvt_pk_bf16_f32 v200, v136, v137
v_cvt_pk_bf16_f32 v201, v138, v139
v_mfma_f32_16x16x32_bf16 v[228:231], a[244:247], v[68:71], v[228:231]
v_cvt_pk_bf16_f32 v202, v152, v153
v_cvt_pk_bf16_f32 v203, v154, v155
v_mfma_f32_16x16x32_bf16 v[180:183], a[248:251], v[68:71], v[180:183]
buffer_store_dwordx4 v[200:203], v8, s[36:39], s52 offen offset:0 sc1
v_mul_f32 v140, v140, v204
v_mfma_f32_16x16x32_bf16 v[244:247], a[252:255], v[68:71], v[244:247]
v_mfma_f32_16x16x32_bf16 v[164:167], v[48:51], v[84:87], v[164:167]
v_mul_f32 v141, v141, v205
v_mul_f32 v142, v142, v206
v_mfma_f32_16x16x32_bf16 v[228:231], v[52:55], v[84:87], v[228:231]
v_mul_f32 v143, v143, v207
v_mul_f32 v156, v156, v220
v_mfma_f32_16x16x32_bf16 v[180:183], v[56:59], v[84:87], v[180:183]
v_mul_f32 v157, v157, v221
v_mul_f32 v158, v158, v222
v_mfma_f32_16x16x32_bf16 v[244:247], v[60:63], v[84:87], v[244:247]
v_mfma_f32_16x16x32_bf16 v[164:167], v[96:99], v[116:119], v[164:167]
v_mul_f32 v159, v159, v223
v_cvt_pk_bf16_f32 v204, v140, v141
v_mfma_f32_16x16x32_bf16 v[228:231], v[100:103], v[116:119], v[228:231]
v_cvt_pk_bf16_f32 v205, v142, v143
v_cvt_pk_bf16_f32 v206, v156, v157
v_mfma_f32_16x16x32_bf16 v[180:183], v[104:107], v[116:119], v[180:183]
v_cvt_pk_bf16_f32 v207, v158, v159
buffer_store_dwordx4 v[204:207], v8, s[36:39], s52 offen offset:2048 sc1
v_mfma_f32_16x16x32_bf16 v[244:247], v[108:111], v[116:119], v[244:247]
v_mfma_f32_16x16x32_bf16 v[168:171], a[224:227], v[40:43], v[168:171]
v_mul_f32 v160, v160, v224
v_mul_f32 v161, v161, v225
v_mfma_f32_16x16x32_bf16 v[232:235], a[228:231], v[40:43], v[232:235]
v_mul_f32 v162, v162, v226
v_mul_f32 v163, v163, v227
v_mfma_f32_16x16x32_bf16 v[184:187], a[232:235], v[40:43], v[184:187]
v_mul_f32 v176, v176, v240
v_mul_f32 v177, v177, v241
v_mfma_f32_16x16x32_bf16 v[248:251], a[236:239], v[40:43], v[248:251]
v_mfma_f32_16x16x32_bf16 v[168:171], a[240:243], v[72:75], v[168:171]
v_mul_f32 v178, v178, v242
v_mul_f32 v179, v179, v243
v_mfma_f32_16x16x32_bf16 v[232:235], a[244:247], v[72:75], v[232:235]
v_cvt_pk_bf16_f32 v224, v160, v161
v_cvt_pk_bf16_f32 v225, v162, v163
v_mfma_f32_16x16x32_bf16 v[184:187], a[248:251], v[72:75], v[184:187]
v_cvt_pk_bf16_f32 v226, v176, v177
v_cvt_pk_bf16_f32 v227, v178, v179
v_mfma_f32_16x16x32_bf16 v[248:251], a[252:255], v[72:75], v[248:251]
v_mfma_f32_16x16x32_bf16 v[168:171], v[48:51], v[88:91], v[168:171]
buffer_store_dwordx4 v[224:227], v7, s[36:39], s53 offen offset:0 sc1
v_mul_f32 v164, v164, v228
v_mfma_f32_16x16x32_bf16 v[232:235], v[52:55], v[88:91], v[232:235]
v_mul_f32 v165, v165, v229
v_mul_f32 v166, v166, v230
v_mfma_f32_16x16x32_bf16 v[184:187], v[56:59], v[88:91], v[184:187]
v_mul_f32 v167, v167, v231
v_mul_f32 v180, v180, v244
v_mfma_f32_16x16x32_bf16 v[248:251], v[60:63], v[88:91], v[248:251]
v_mfma_f32_16x16x32_bf16 v[168:171], v[96:99], v[120:123], v[168:171]
v_mul_f32 v181, v181, v245
v_mul_f32 v182, v182, v246
v_mfma_f32_16x16x32_bf16 v[232:235], v[100:103], v[120:123], v[232:235]
v_mul_f32 v183, v183, v247
v_cvt_pk_bf16_f32 v228, v164, v165
v_mfma_f32_16x16x32_bf16 v[184:187], v[104:107], v[120:123], v[184:187]
v_cvt_pk_bf16_f32 v229, v166, v167
v_cvt_pk_bf16_f32 v230, v180, v181
v_mfma_f32_16x16x32_bf16 v[248:251], v[108:111], v[120:123], v[248:251]
v_mfma_f32_16x16x32_bf16 v[172:175], a[224:227], v[44:47], v[172:175]
v_cvt_pk_bf16_f32 v231, v182, v183
buffer_store_dwordx4 v[228:231], v7, s[36:39], s53 offen offset:2048 sc1
v_mfma_f32_16x16x32_bf16 v[236:239], a[228:231], v[44:47], v[236:239]
v_mfma_f32_16x16x32_bf16 v[188:191], a[232:235], v[44:47], v[188:191]
v_mfma_f32_16x16x32_bf16 v[252:255], a[236:239], v[44:47], v[252:255]
v_mfma_f32_16x16x32_bf16 v[172:175], a[240:243], v[76:79], v[172:175]
v_mul_f32 v168, v168, v232
v_mul_f32 v169, v169, v233
v_mfma_f32_16x16x32_bf16 v[236:239], a[244:247], v[76:79], v[236:239]
v_mul_f32 v170, v170, v234
v_mul_f32 v171, v171, v235
v_mfma_f32_16x16x32_bf16 v[188:191], a[248:251], v[76:79], v[188:191]
v_mul_f32 v184, v184, v248
v_mul_f32 v185, v185, v249
v_mfma_f32_16x16x32_bf16 v[252:255], a[252:255], v[76:79], v[252:255]
v_mfma_f32_16x16x32_bf16 v[172:175], v[48:51], v[92:95], v[172:175]
v_mul_f32 v186, v186, v250
v_mul_f32 v187, v187, v251
v_mfma_f32_16x16x32_bf16 v[236:239], v[52:55], v[92:95], v[236:239]
v_cvt_pk_bf16_f32 v232, v168, v169
v_cvt_pk_bf16_f32 v233, v170, v171
v_mfma_f32_16x16x32_bf16 v[188:191], v[56:59], v[92:95], v[188:191]
v_cvt_pk_bf16_f32 v234, v184, v185
v_cvt_pk_bf16_f32 v235, v186, v187
v_mfma_f32_16x16x32_bf16 v[252:255], v[60:63], v[92:95], v[252:255]
v_mfma_f32_16x16x32_bf16 v[172:175], v[96:99], v[124:127], v[172:175]
buffer_store_dwordx4 v[232:235], v8, s[36:39], s53 offen offset:0 sc1
v_mfma_f32_16x16x32_bf16 v[236:239], v[100:103], v[124:127], v[236:239]
v_mfma_f32_16x16x32_bf16 v[188:191], v[104:107], v[124:127], v[188:191]
v_mfma_f32_16x16x32_bf16 v[252:255], v[108:111], v[124:127], v[252:255]
s_mov_b64 exec, 1
s_waitcnt vmcnt(3)
buffer_atomic_add v9, off, s[36:39], s55 sc1
s_mov_b64 exec, -1
s_nop 4
v_mul_f32 v172, v172, v236
v_mul_f32 v173, v173, v237
v_mul_f32 v174, v174, v238
v_mul_f32 v175, v175, v239
v_mul_f32 v188, v188, v252
v_mul_f32 v189, v189, v253
v_mul_f32 v190, v190, v254
v_mul_f32 v191, v191, v255
v_cvt_pk_bf16_f32 v236, v172, v173
v_cvt_pk_bf16_f32 v237, v174, v175
v_cvt_pk_bf16_f32 v238, v188, v189
v_cvt_pk_bf16_f32 v239, v190, v191
buffer_store_dwordx4 v[236:239], v8, s[36:39], s53 offen offset:2048 sc1
s_waitcnt lgkmcnt(0)
s_add_u32 m0, s45, 8192
s_lshl3_add_u32 s63, s7, s58
s_nop 0
buffer_load_dwordx4 v20, s[24:27], s63 offen offset:0 lds
buffer_load_dwordx4 v21, s[24:27], s63 offen offset:1024 lds
buffer_load_dwordx4 v22, s[24:27], s63 offen offset:2048 lds
buffer_load_dwordx4 v23, s[24:27], s63 offen offset:3072 lds
ds_read_b64_tr_b16 a[16:17], v24 offset:16384
ds_read_b64_tr_b16 a[18:19], v24 offset:18432
ds_read_b64_tr_b16 a[20:21], v25 offset:16384
ds_read_b64_tr_b16 a[22:23], v25 offset:18432
ds_read_b64_tr_b16 a[24:25], v26 offset:16384
ds_read_b64_tr_b16 a[26:27], v26 offset:18432
ds_read_b64_tr_b16 a[28:29], v27 offset:16384
ds_read_b64_tr_b16 a[30:31], v27 offset:18432
ds_read_b64_tr_b16 a[128:129], v24 offset:4096
ds_read_b64_tr_b16 a[130:131], v24 offset:6144
ds_read_b64_tr_b16 a[132:133], v25 offset:4096
ds_read_b64_tr_b16 a[134:135], v25 offset:6144
ds_read_b64_tr_b16 a[136:137], v26 offset:4096
ds_read_b64_tr_b16 a[138:139], v26 offset:6144
ds_read_b64_tr_b16 a[140:141], v27 offset:4096
ds_read_b64_tr_b16 a[142:143], v27 offset:6144
ds_read_b64_tr_b16 a[32:33], v24 offset:0
ds_read_b64_tr_b16 a[34:35], v24 offset:2048
ds_read_b64_tr_b16 a[36:37], v25 offset:0
ds_read_b64_tr_b16 a[38:39], v25 offset:2048
ds_read_b64_tr_b16 a[40:41], v26 offset:0
ds_read_b64_tr_b16 a[42:43], v26 offset:2048
ds_read_b64_tr_b16 a[44:45], v27 offset:0
ds_read_b64_tr_b16 a[46:47], v27 offset:2048
ds_read_b64_tr_b16 a[144:145], v24 offset:24576
ds_read_b64_tr_b16 a[146:147], v24 offset:26624
ds_read_b64_tr_b16 a[148:149], v25 offset:24576
ds_read_b64_tr_b16 a[150:151], v25 offset:26624
ds_read_b64_tr_b16 a[152:153], v26 offset:24576
ds_read_b64_tr_b16 a[154:155], v26 offset:26624
ds_read_b64_tr_b16 a[156:157], v27 offset:24576
ds_read_b64_tr_b16 a[158:159], v27 offset:26624
ds_read_b64_tr_b16 a[48:49], v24 offset:20480
ds_read_b64_tr_b16 a[50:51], v24 offset:22528
ds_read_b64_tr_b16 a[52:53], v25 offset:20480
ds_read_b64_tr_b16 a[54:55], v25 offset:22528
ds_read_b64_tr_b16 a[56:57], v26 offset:20480
ds_read_b64_tr_b16 a[58:59], v26 offset:22528
ds_read_b64_tr_b16 a[60:61], v27 offset:20480
ds_read_b64_tr_b16 a[62:63], v27 offset:22528
v_lshrrev_b32 v2, 3, v0
v_and_b32 v3, 7, v0
v_lshlrev_b32 v3, 4, v3
v_lshl_add_u32 v31, v2, 8, v3
v_lshlrev_b32 v13, 4, v0
s_add_u32 s60, s44, 1
s_and_b32 s60, s60, 3
v_lshl_add_u32 v2, s60, 11, v31
s_mul_i32 s60, s60, 12288
v_add_u32 v10, s60, v13
s_add_u32 s60, s44, 2
s_and_b32 s60, s60, 3
v_lshl_add_u32 v3, s60, 11, v31
s_mul_i32 s60, s60, 12288
v_add_u32 v11, s60, v13
s_add_u32 s60, s44, 3
s_and_b32 s60, s60, 3
v_lshl_add_u32 v4, s60, 11, v31
s_mul_i32 s60, s60, 12288
v_add_u32 v12, s60, v13
s_add_u32 s60, s44, 4
s_and_b32 s60, s60, 3
v_lshl_add_u32 v5, s60, 11, v31
s_mul_i32 s60, s60, 12288
v_add_u32 v13, s60, v13
v_and_b32 v14, 15, v0
v_bfe_u32 v15, v0, 4, 1
v_lshlrev_b32 v15, 5, v15
v_lshl_add_u32 v14, v14, 10, v15
v_lshrrev_b32 v15, 5, v0
v_lshl_add_u32 v14, v15, 4, v14
s_add_u32 s47, s4, s54
s_add_u32 s48, s5, s54
s_add_u32 s49, s6, s54
s_add_u32 s50, s7, s54
s_add_u32 s34, s8, s54
s_add_u32 s35, s9, s54
s_add_u32 s3, s10, s54
s_mov_b32 s61, 65536
s_waitcnt lgkmcnt(0)
s_add_u32 m0, s45, 12288
s_lshl3_add_u32 s63, s8, s51
s_nop 0
buffer_load_dwordx4 v20, s[24:27], s63 offen offset:0 lds
buffer_load_dwordx4 v21, s[24:27], s63 offen offset:1024 lds
buffer_load_dwordx4 v22, s[24:27], s63 offen offset:2048 lds
buffer_load_dwordx4 v23, s[24:27], s63 offen offset:3072 lds
s_mov_b64 exec, 1
s_waitcnt vmcnt(8)
v_mov_b32 v31, 256
buffer_atomic_add v31, off, s[36:39], s55 sc1
v_mov_b32 v29, 0
buffer_atomic_add v29, off, s[36:39], s56 sc0 sc1
s_mov_b64 exec, -1
s_add_u32 m0, s45, 16384
s_lshl3_add_u32 s63, s8, s58
s_nop 0
buffer_load_dwordx4 v20, s[24:27], s63 offen offset:0 lds
buffer_load_dwordx4 v21, s[24:27], s63 offen offset:1024 lds
buffer_load_dwordx4 v22, s[24:27], s63 offen offset:2048 lds
buffer_load_dwordx4 v23, s[24:27], s63 offen offset:3072 lds
s_add_u32 m0, s45, 4096
s_lshl3_add_u32 s63, s9, s51
s_nop 0
buffer_load_dwordx4 v20, s[24:27], s63 offen offset:0 lds
buffer_load_dwordx4 v21, s[24:27], s63 offen offset:1024 lds
buffer_load_dwordx4 v22, s[24:27], s63 offen offset:2048 lds
buffer_load_dwordx4 v23, s[24:27], s63 offen offset:3072 lds
s_add_u32 m0, s45, 0
s_lshl3_add_u32 s63, s9, s58
s_nop 0
buffer_load_dwordx4 v20, s[24:27], s63 offen offset:0 lds
buffer_load_dwordx4 v21, s[24:27], s63 offen offset:1024 lds
buffer_load_dwordx4 v22, s[24:27], s63 offen offset:2048 lds
buffer_load_dwordx4 v23, s[24:27], s63 offen offset:3072 lds
s_add_u32 m0, s45, 24576
s_lshl3_add_u32 s63, s10, s51
s_nop 0
buffer_load_dwordx4 v20, s[24:27], s63 offen offset:0 lds
buffer_load_dwordx4 v21, s[24:27], s63 offen offset:1024 lds
buffer_load_dwordx4 v22, s[24:27], s63 offen offset:2048 lds
buffer_load_dwordx4 v23, s[24:27], s63 offen offset:3072 lds
s_add_u32 m0, s45, 20480
s_lshl3_add_u32 s63, s10, s58
s_nop 0
buffer_load_dwordx4 v20, s[24:27], s63 offen offset:0 lds
buffer_load_dwordx4 v21, s[24:27], s63 offen offset:1024 lds
buffer_load_dwordx4 v22, s[24:27], s63 offen offset:2048 lds
buffer_load_dwordx4 v23, s[24:27], s63 offen offset:3072 lds
s_mov_b64 exec, 1
s_waitcnt vmcnt(20)
v_readfirstlane_b32 s60, v29
s_and_b32 s60, s60, 0xff
s_cmp_eq_u32 s60, 7
s_cbranch_scc1 .L_go_%=
v_mov_b32 v28, 0
buffer_atomic_add v28, off, s[36:39], s56 sc0 sc1
s_sleep 6
s_branch .L_poll_%=
.L_helper_%=:
s_cmp_lg_u32 s2, 255
s_cbranch_scc1 .L_member_%=
s_lshl_b32 s60, s44, 6
v_add_u32 v40, s60, v0
v_cmp_gt_u32 vcc, 128, v40
v_lshlrev_b32 v40, 7, v40
v_mov_b32 v52, 0x80000000
v_cndmask_b32 v41, v52, v40, vcc
s_mov_b32 s60, 15728640
s_mov_b32 s61, 15761408
buffer_store_dword v9, v40, s[36:39], s60 offen offset:64 sc1
buffer_store_dword v9, v41, s[36:39], s61 offen sc1
v_xor_b32 v42, 32640, v40
v_mov_b32 v44, 1
s_waitcnt vmcnt(0)
s_barrier
buffer_store_dword v44, v40, s[36:39], s60 offen sc1
s_branch .L_pfend_%=
.L_member_%=:
s_and_b32 s60, s2, 7
s_lshr_b32 s52, s2, 3
s_sub_u32 s52, s52, 28
s_lshl2_add_u32 s52, s52, s44
s_lshl_b32 s55, s60, 18
s_and_b32 s47, s60, 3
s_mul_i32 s47, s47, 896
s_cmp_eq_u32 s60, 7
s_cselect_b32 s48, 12, 16
s_cmp_ge_u32 s60, 4
s_cselect_b32 s20, s8, s6
s_cselect_b32 s21, s9, s7
s_and_b32 s21, s21, 0xffff
v_lshlrev_b32 v32, 10, v0
v_mul_u32_u24 v33, 3584, v0
.L_pf_%=:
s_mul_i32 s60, s52, 187
s_lshr_b32 s60, s60, 11
s_mul_i32 s53, s60, 11
s_sub_u32 s53, s52, s53
s_add_u32 s60, s60, 1
s_cmp_ge_u32 s60, 8
s_cbranch_scc1 .L_pfend_%=
s_cmp_ge_u32 s53, 7
s_cbranch_scc1 .L_pfx_%=
s_mul_i32 s60, s60, 229376
s_lshl_b32 s53, s53, 7
s_add_u32 s60, s60, s53
s_add_u32 s60, s60, s47
buffer_load_dword v31, v33, s[20:23], s60 offen
s_branch .L_pfn_%=
.L_pfx_%=:
s_lshl_b32 s60, s60, 7
s_sub_u32 s53, s53, 7
s_lshl_b32 s53, s53, 16
s_add_u32 s60, s60, s53
s_add_u32 s60, s60, s55
buffer_load_dword v31, v32, s[16:19], s60 offen
.L_pfn_%=:
s_add_u32 s52, s52, s48
s_cmp_lt_u32 s52, 80
s_cbranch_scc1 .L_pf_%=
.L_pfend_%=:
s_mov_b32 s24, s10
s_and_b32 s25, s11, 0xffff
s_mov_b32 s28, s12
s_and_b32 s29, s13, 0xffff
s_mov_b32 s26, 1835008
s_mov_b32 s27, 0x20000
s_mov_b32 s30, 2097152
s_mov_b32 s31, 0x20000
v_lshrrev_b32 v32, 3, v0
v_and_b32 v33, 7, v0
v_lshrrev_b32 v34, 4, v0
v_bfe_u32 v35, v0, 3, 2
v_and_b32 v36, 3, v0
v_lshlrev_b32 v36, 3, v36
v_lshl_add_u32 v35, v35, 5, v36
v_lshrrev_b32 v36, 2, v0
v_lshl_add_u32 v36, v36, 7, v35
v_add_u32 v24, s45, v36
v_xor_b32 v25, 32, v24
v_xor_b32 v26, 64, v24
v_xor_b32 v27, 96, v24
v_lshlrev_b32 v35, 1, v34
v_xor_b32 v35, v33, v35
v_lshlrev_b32 v35, 4, v35
v_lshl_add_u32 v20, v32, 10, v35
v_add_u32 v21, 7168, v20
v_add_u32 v22, 14336, v20
v_add_u32 v23, 21504, v20
s_and_b32 s60, s2, 7
s_lshr_b32 s61, s2, 3
s_and_b32 s62, s60, 1
s_mov_b32 s51, 3
s_sub_u32 s61, s61, 28
s_lshl2_add_u32 s61, s62, s61
s_lshr_b32 s60, s60, 1
s_lshl3_add_u32 s60, s60, s61
s_and_b32 s61, s61, 3
s_lshl_b32 s61, s61, 1
s_lshl_b32 s51, s51, 7
s_lshl2_add_u32 s56, s60, s44
s_lshl_b32 s59, s56, 14
s_add_u32 s59, s59, s51
s_mul_i32 s54, s56, 114688
s_lshl_b32 s56, s56, 7
s_add_u32 s56, s56, 15761408
s_mul_i32 s62, s44, 458752
s_add_u32 s51, s51, s62
s_add_u32 s58, s51, 32768
s_lshl_b32 s61, s61, 13
s_cmp_ge_u32 s61, 57344
s_cselect_b32 s4, 0, s61
s_add_u32 s61, s4, 8192
s_cmp_ge_u32 s61, 57344
s_cselect_b32 s5, 0, s61
s_add_u32 s61, s5, 8192
s_cmp_ge_u32 s61, 57344
s_cselect_b32 s6, 0, s61
s_add_u32 s61, s6, 8192
s_cmp_ge_u32 s61, 57344
s_cselect_b32 s7, 0, s61
s_add_u32 s61, s7, 8192
s_cmp_ge_u32 s61, 57344
s_cselect_b32 s8, 0, s61
s_add_u32 s61, s8, 8192
s_cmp_ge_u32 s61, 57344
s_cselect_b32 s9, 0, s61
s_add_u32 s61, s9, 8192
s_cmp_ge_u32 s61, 57344
s_cselect_b32 s10, 0, s61
s_lshr_b32 s60, s2, 3
s_sub_u32 s60, s60, 28
s_lshl2_add_u32 s60, s60, s44
s_and_b32 s61, s2, 7
s_mul_i32 s61, s61, 28
s_add_u32 s60, s60, s61
s_lshl_b32 s60, s60, 13
v_lshlrev_b32 v32, 7, v0
buffer_load_dword v31, v32, s[24:27], s60 offen
s_add_u32 s60, s60, 131072
s_cmp_ge_u32 s60, 1835008
s_cselect_b32 s61, 1835008, 0
s_sub_u32 s60, s60, s61
buffer_load_dword v31, v32, s[24:27], s60 offen
s_add_u32 m0, s45, 0
s_lshl3_add_u32 s63, s4, s51
s_nop 0
buffer_load_dwordx4 v20, s[24:27], s63 offen offset:0 lds
buffer_load_dwordx4 v21, s[24:27], s63 offen offset:1024 lds
buffer_load_dwordx4 v22, s[24:27], s63 offen offset:2048 lds
buffer_load_dwordx4 v23, s[24:27], s63 offen offset:3072 lds
s_add_u32 m0, s45, 4096
s_lshl3_add_u32 s63, s4, s58
s_nop 0
buffer_load_dwordx4 v20, s[24:27], s63 offen offset:0 lds
buffer_load_dwordx4 v21, s[24:27], s63 offen offset:1024 lds
buffer_load_dwordx4 v22, s[24:27], s63 offen offset:2048 lds
buffer_load_dwordx4 v23, s[24:27], s63 offen offset:3072 lds
s_add_u32 m0, s45, 8192
s_lshl3_add_u32 s63, s5, s51
s_nop 0
buffer_load_dwordx4 v20, s[24:27], s63 offen offset:0 lds
buffer_load_dwordx4 v21, s[24:27], s63 offen offset:1024 lds
buffer_load_dwordx4 v22, s[24:27], s63 offen offset:2048 lds
buffer_load_dwordx4 v23, s[24:27], s63 offen offset:3072 lds
s_waitcnt vmcnt(8)
ds_read_b64_tr_b16 a[0:1], v24 offset:0
ds_read_b64_tr_b16 a[2:3], v24 offset:2048
ds_read_b64_tr_b16 a[4:5], v25 offset:0
ds_read_b64_tr_b16 a[6:7], v25 offset:2048
ds_read_b64_tr_b16 a[8:9], v26 offset:0
ds_read_b64_tr_b16 a[10:11], v26 offset:2048
ds_read_b64_tr_b16 a[12:13], v27 offset:0
ds_read_b64_tr_b16 a[14:15], v27 offset:2048
s_add_u32 m0, s45, 12288
s_lshl3_add_u32 s63, s5, s58
s_nop 0
buffer_load_dwordx4 v20, s[24:27], s63 offen offset:0 lds
buffer_load_dwordx4 v21, s[24:27], s63 offen offset:1024 lds
buffer_load_dwordx4 v22, s[24:27], s63 offen offset:2048 lds
buffer_load_dwordx4 v23, s[24:27], s63 offen offset:3072 lds
buffer_load_dword v9, v6, s[36:39], s57 offen sc1
s_waitcnt vmcnt(9)
ds_read_b64_tr_b16 a[112:113], v24 offset:4096
ds_read_b64_tr_b16 a[114:115], v24 offset:6144
ds_read_b64_tr_b16 a[116:117], v25 offset:4096
ds_read_b64_tr_b16 a[118:119], v25 offset:6144
ds_read_b64_tr_b16 a[120:121], v26 offset:4096
ds_read_b64_tr_b16 a[122:123], v26 offset:6144
ds_read_b64_tr_b16 a[124:125], v27 offset:4096
ds_read_b64_tr_b16 a[126:127], v27 offset:6144
s_add_u32 m0, s45, 16384
s_lshl3_add_u32 s63, s6, s51
s_nop 0
buffer_load_dwordx4 v20, s[24:27], s63 offen offset:0 lds
buffer_load_dwordx4 v21, s[24:27], s63 offen offset:1024 lds
buffer_load_dwordx4 v22, s[24:27], s63 offen offset:2048 lds
buffer_load_dwordx4 v23, s[24:27], s63 offen offset:3072 lds
s_waitcnt vmcnt(9)
ds_read_b64_tr_b16 a[16:17], v24 offset:8192
ds_read_b64_tr_b16 a[18:19], v24 offset:10240
ds_read_b64_tr_b16 a[20:21], v25 offset:8192
ds_read_b64_tr_b16 a[22:23], v25 offset:10240
ds_read_b64_tr_b16 a[24:25], v26 offset:8192
ds_read_b64_tr_b16 a[26:27], v26 offset:10240
ds_read_b64_tr_b16 a[28:29], v27 offset:8192
ds_read_b64_tr_b16 a[30:31], v27 offset:10240
s_add_u32 m0, s45, 20480
s_lshl3_add_u32 s63, s6, s58
s_nop 0
buffer_load_dwordx4 v20, s[24:27], s63 offen offset:0 lds
buffer_load_dwordx4 v21, s[24:27], s63 offen offset:1024 lds
buffer_load_dwordx4 v22, s[24:27], s63 offen offset:2048 lds
buffer_load_dwordx4 v23, s[24:27], s63 offen offset:3072 lds
s_waitcnt vmcnt(8)
v_readfirstlane_b32 s60, v9
v_mov_b32 v9, 1
s_or_b32 s60, s60, s44
s_cbranch_scc0 .L_ackslow_h_%=
.L_ackok_h_%=:
buffer_store_dword v9, v6, s[36:39], s57 offen offset:64 sc1
ds_read_b64_tr_b16 a[128:129], v24 offset:12288
ds_read_b64_tr_b16 a[130:131], v24 offset:14336
ds_read_b64_tr_b16 a[132:133], v25 offset:12288
ds_read_b64_tr_b16 a[134:135], v25 offset:14336
ds_read_b64_tr_b16 a[136:137], v26 offset:12288
ds_read_b64_tr_b16 a[138:139], v26 offset:14336
ds_read_b64_tr_b16 a[140:141], v27 offset:12288
ds_read_b64_tr_b16 a[142:143], v27 offset:14336
s_add_u32 m0, s45, 24576
s_lshl3_add_u32 s63, s7, s51
s_nop 0
buffer_load_dwordx4 v20, s[24:27], s63 offen offset:0 lds
buffer_load_dwordx4 v21, s[24:27], s63 offen offset:1024 lds
buffer_load_dwordx4 v22, s[24:27], s63 offen offset:2048 lds
buffer_load_dwordx4 v23, s[24:27], s63 offen offset:3072 lds
s_waitcnt vmcnt(9)
ds_read_b64_tr_b16 a[32:33], v24 offset:16384
ds_read_b64_tr_b16 a[34:35], v24 offset:18432
ds_read_b64_tr_b16 a[36:37], v25 offset:16384
ds_read_b64_tr_b16 a[38:39], v25 offset:18432
ds_read_b64_tr_b16 a[40:41], v26 offset:16384
ds_read_b64_tr_b16 a[42:43], v26 offset:18432
ds_read_b64_tr_b16 a[44:45], v27 offset:16384
ds_read_b64_tr_b16 a[46:47], v27 offset:18432
s_waitcnt lgkmcnt(14)
s_add_u32 m0, s45, 0
s_lshl3_add_u32 s63, s9, s58
s_nop 0
buffer_load_dwordx4 v20, s[24:27], s63 offen offset:0 lds
buffer_load_dwordx4 v21, s[24:27], s63 offen offset:1024 lds
buffer_load_dwordx4 v22, s[24:27], s63 offen offset:2048 lds
buffer_load_dwordx4 v23, s[24:27], s63 offen offset:3072 lds
s_waitcnt vmcnt(9)
ds_read_b64_tr_b16 a[144:145], v24 offset:20480
ds_read_b64_tr_b16 a[146:147], v24 offset:22528
ds_read_b64_tr_b16 a[148:149], v25 offset:20480
ds_read_b64_tr_b16 a[150:151], v25 offset:22528
ds_read_b64_tr_b16 a[152:153], v26 offset:20480
ds_read_b64_tr_b16 a[154:155], v26 offset:22528
ds_read_b64_tr_b16 a[156:157], v27 offset:20480
ds_read_b64_tr_b16 a[158:159], v27 offset:22528
s_add_u32 m0, s45, 4096
s_lshl3_add_u32 s63, s9, s51
s_nop 0
buffer_load_dwordx4 v20, s[24:27], s63 offen offset:0 lds
buffer_load_dwordx4 v21, s[24:27], s63 offen offset:1024 lds
buffer_load_dwordx4 v22, s[24:27], s63 offen offset:2048 lds
buffer_load_dwordx4 v23, s[24:27], s63 offen offset:3072 lds
s_waitcnt vmcnt(8)
ds_read_b64_tr_b16 a[48:49], v24 offset:24576
ds_read_b64_tr_b16 a[50:51], v24 offset:26624
ds_read_b64_tr_b16 a[52:53], v25 offset:24576
ds_read_b64_tr_b16 a[54:55], v25 offset:26624
ds_read_b64_tr_b16 a[56:57], v26 offset:24576
ds_read_b64_tr_b16 a[58:59], v26 offset:26624
ds_read_b64_tr_b16 a[60:61], v27 offset:24576
ds_read_b64_tr_b16 a[62:63], v27 offset:26624
s_add_u32 m0, s45, 8192
s_lshl3_add_u32 s63, s7, s58
s_nop 0
buffer_load_dwordx4 v20, s[24:27], s63 offen offset:0 lds
buffer_load_dwordx4 v21, s[24:27], s63 offen offset:1024 lds
buffer_load_dwordx4 v22, s[24:27], s63 offen offset:2048 lds
buffer_load_dwordx4 v23, s[24:27], s63 offen offset:3072 lds
s_waitcnt lgkmcnt(14)
s_add_u32 m0, s45, 12288
s_lshl3_add_u32 s63, s8, s51
s_nop 0
buffer_load_dwordx4 v20, s[24:27], s63 offen offset:0 lds
buffer_load_dwordx4 v21, s[24:27], s63 offen offset:1024 lds
buffer_load_dwordx4 v22, s[24:27], s63 offen offset:2048 lds
buffer_load_dwordx4 v23, s[24:27], s63 offen offset:3072 lds
s_add_u32 m0, s45, 16384
s_lshl3_add_u32 s63, s8, s58
s_nop 0
buffer_load_dwordx4 v20, s[24:27], s63 offen offset:0 lds
buffer_load_dwordx4 v21, s[24:27], s63 offen offset:1024 lds
buffer_load_dwordx4 v22, s[24:27], s63 offen offset:2048 lds
buffer_load_dwordx4 v23, s[24:27], s63 offen offset:3072 lds
s_waitcnt lgkmcnt(8)
s_add_u32 m0, s45, 20480
s_lshl3_add_u32 s63, s10, s58
s_nop 0
buffer_load_dwordx4 v20, s[24:27], s63 offen offset:0 lds
buffer_load_dwordx4 v21, s[24:27], s63 offen offset:1024 lds
buffer_load_dwordx4 v22, s[24:27], s63 offen offset:2048 lds
buffer_load_dwordx4 v23, s[24:27], s63 offen offset:3072 lds
s_waitcnt lgkmcnt(0)
s_add_u32 m0, s45, 24576
s_lshl3_add_u32 s63, s10, s51
s_nop 0
buffer_load_dwordx4 v20, s[24:27], s63 offen offset:0 lds
buffer_load_dwordx4 v21, s[24:27], s63 offen offset:1024 lds
buffer_load_dwordx4 v22, s[24:27], s63 offen offset:2048 lds
buffer_load_dwordx4 v23, s[24:27], s63 offen offset:3072 lds
s_waitcnt vmcnt(0)
s_waitcnt lgkmcnt(0)
s_cmp_lg_u32 s2, 255
s_cbranch_scc1 .L_member2_%=
s_mov_b32 s60, 15728640
s_mov_b32 s61, 65536
.L_sweep_%=:
buffer_load_dword v48, v40, s[36:39], s60 offen offset:64 sc1
s_waitcnt vmcnt(0)
v_cmp_ne_u32 vcc, 1, v48
v_cmp_ne_u32 s[40:41], 0, v42
s_and_b64 vcc, vcc, s[40:41]
s_cbranch_vccz .L_hjoin_%=
v_cndmask_b32 v43, v52, v40, vcc
buffer_store_dword v44, v43, s[36:39], s60 offen sc1
s_sleep 4
s_sub_u32 s61, s61, 1
s_cbranch_scc0 .L_sweep_%=
s_branch .L_hjoin_%=
.L_member2_%=:
s_barrier
.L_hjoin_%=:
v_lshrrev_b32 v2, 3, v0
v_and_b32 v3, 7, v0
v_lshlrev_b32 v3, 4, v3
v_lshl_add_u32 v31, v2, 8, v3
v_lshlrev_b32 v13, 4, v0
s_add_u32 s60, s44, 1
s_and_b32 s60, s60, 3
v_lshl_add_u32 v2, s60, 11, v31
s_mul_i32 s60, s60, 12288
v_add_u32 v10, s60, v13
s_add_u32 s60, s44, 2
s_and_b32 s60, s60, 3
v_lshl_add_u32 v3, s60, 11, v31
s_mul_i32 s60, s60, 12288
v_add_u32 v11, s60, v13
s_add_u32 s60, s44, 3
s_and_b32 s60, s60, 3
v_lshl_add_u32 v4, s60, 11, v31
s_mul_i32 s60, s60, 12288
v_add_u32 v12, s60, v13
s_add_u32 s60, s44, 4
s_and_b32 s60, s60, 3
v_lshl_add_u32 v5, s60, 11, v31
s_mul_i32 s60, s60, 12288
v_add_u32 v13, s60, v13
v_and_b32 v14, 15, v0
v_bfe_u32 v15, v0, 4, 1
v_lshlrev_b32 v15, 5, v15
v_lshl_add_u32 v14, v14, 10, v15
v_lshrrev_b32 v15, 5, v0
v_lshl_add_u32 v14, v15, 4, v14
s_add_u32 s47, s4, s54
s_add_u32 s48, s5, s54
s_add_u32 s49, s6, s54
s_add_u32 s50, s7, s54
s_add_u32 s34, s8, s54
s_add_u32 s35, s9, s54
s_add_u32 s3, s10, s54
s_mov_b32 s61, 65536
s_mov_b64 exec, 1
v_mov_b32 v28, 0
buffer_atomic_add v28, off, s[36:39], s56 sc0 sc1
s_sleep 6
.L_poll_%=:
v_mov_b32 v29, 0
buffer_atomic_add v29, off, s[36:39], s56 sc0 sc1
s_waitcnt vmcnt(1)
v_readfirstlane_b32 s60, v28
s_and_b32 s60, s60, 0xff
s_cmp_eq_u32 s60, 7
s_cbranch_scc1 .L_go_%=
v_mov_b32 v28, 0
buffer_atomic_add v28, off, s[36:39], s56 sc0 sc1
s_waitcnt vmcnt(1)
v_readfirstlane_b32 s60, v29
s_and_b32 s60, s60, 0xff
s_cmp_eq_u32 s60, 7
s_cbranch_scc1 .L_go_%=
s_sub_u32 s61, s61, 1
s_cbranch_scc0 .L_poll_%=
.L_go_%=:
s_mov_b64 exec, -1
buffer_load_dwordx4 v[160:163], v2, s[36:39], s47 offen sc1
buffer_load_dwordx4 v[164:167], v3, s[36:39], s47 offen sc1
buffer_load_dwordx4 v[168:171], v4, s[36:39], s47 offen sc1
buffer_load_dwordx4 v[172:175], v5, s[36:39], s47 offen sc1
s_add_u32 s47, s47, 57344
buffer_load_dwordx4 v[176:179], v2, s[36:39], s48 offen sc1
buffer_load_dwordx4 v[180:183], v3, s[36:39], s48 offen sc1
buffer_load_dwordx4 v[184:187], v4, s[36:39], s48 offen sc1
buffer_load_dwordx4 v[188:191], v5, s[36:39], s48 offen sc1
s_add_u32 s48, s48, 57344
buffer_load_dwordx4 v[192:195], v2, s[36:39], s49 offen sc1
s_waitcnt vmcnt(8)
v_mfma_f32_16x16x32_bf16 v[32:35], a[0:3], v[160:163], 0
buffer_load_dwordx4 v[196:199], v3, s[36:39], s49 offen sc1
ds_read_b64_tr_b16 a[64:65], v24 offset:12288
ds_read_b64_tr_b16 a[66:67], v24 offset:14336
v_mfma_f32_16x16x32_bf16 v[36:39], a[4:7], v[160:163], 0
ds_read_b64_tr_b16 a[68:69], v25 offset:12288
ds_read_b64_tr_b16 a[70:71], v25 offset:14336
v_mfma_f32_16x16x32_bf16 v[40:43], a[8:11], v[160:163], 0
ds_read_b64_tr_b16 a[72:73], v26 offset:12288
ds_read_b64_tr_b16 a[74:75], v26 offset:14336
v_mfma_f32_16x16x32_bf16 v[44:47], a[12:15], v[160:163], 0
ds_read_b64_tr_b16 a[76:77], v27 offset:12288
ds_read_b64_tr_b16 a[78:79], v27 offset:14336
s_waitcnt vmcnt(8)
v_mfma_f32_16x16x32_bf16 v[60:63], a[12:15], v[164:167], 0
buffer_load_dwordx4 v[200:203], v4, s[36:39], s49 offen sc1
ds_read_b64_tr_b16 a[80:81], v24 offset:4096
ds_read_b64_tr_b16 a[82:83], v24 offset:6144
v_mfma_f32_16x16x32_bf16 v[56:59], a[8:11], v[164:167], 0
ds_read_b64_tr_b16 a[84:85], v25 offset:4096
ds_read_b64_tr_b16 a[86:87], v25 offset:6144
v_mfma_f32_16x16x32_bf16 v[52:55], a[4:7], v[164:167], 0
ds_read_b64_tr_b16 a[88:89], v26 offset:4096
ds_read_b64_tr_b16 a[90:91], v26 offset:6144
v_mfma_f32_16x16x32_bf16 v[48:51], a[0:3], v[164:167], 0
ds_read_b64_tr_b16 a[92:93], v27 offset:4096
ds_read_b64_tr_b16 a[94:95], v27 offset:6144
s_waitcnt vmcnt(8)
v_mfma_f32_16x16x32_bf16 v[64:67], a[0:3], v[168:171], 0
buffer_load_dwordx4 v[204:207], v5, s[36:39], s49 offen sc1
s_add_u32 s49, s49, 57344
ds_read_b64_tr_b16 a[96:97], v24 offset:24576
ds_read_b64_tr_b16 a[98:99], v24 offset:26624
v_mfma_f32_16x16x32_bf16 v[68:71], a[4:7], v[168:171], 0
ds_read_b64_tr_b16 a[100:101], v25 offset:24576
ds_read_b64_tr_b16 a[102:103], v25 offset:26624
v_mfma_f32_16x16x32_bf16 v[72:75], a[8:11], v[168:171], 0
ds_read_b64_tr_b16 a[104:105], v26 offset:24576
ds_read_b64_tr_b16 a[106:107], v26 offset:26624
v_mfma_f32_16x16x32_bf16 v[76:79], a[12:15], v[168:171], 0
ds_read_b64_tr_b16 a[108:109], v27 offset:24576
ds_read_b64_tr_b16 a[110:111], v27 offset:26624
s_waitcnt vmcnt(8)
v_mfma_f32_16x16x32_bf16 v[92:95], a[12:15], v[172:175], 0
buffer_load_dwordx4 v[208:211], v2, s[36:39], s50 offen sc1
ds_read_b64_tr_b16 a[160:161], v24 offset:8192
ds_read_b64_tr_b16 a[162:163], v24 offset:10240
v_mfma_f32_16x16x32_bf16 v[88:91], a[8:11], v[172:175], 0
ds_read_b64_tr_b16 a[164:165], v25 offset:8192
ds_read_b64_tr_b16 a[166:167], v25 offset:10240
v_mfma_f32_16x16x32_bf16 v[84:87], a[4:7], v[172:175], 0
ds_read_b64_tr_b16 a[168:169], v26 offset:8192
ds_read_b64_tr_b16 a[170:171], v26 offset:10240
v_mfma_f32_16x16x32_bf16 v[80:83], a[0:3], v[172:175], 0
ds_read_b64_tr_b16 a[172:173], v27 offset:8192
ds_read_b64_tr_b16 a[174:175], v27 offset:10240
s_waitcnt vmcnt(8)
v_mfma_f32_16x16x32_bf16 v[32:35], a[16:19], v[176:179], v[32:35]
buffer_load_dwordx4 v[212:215], v3, s[36:39], s50 offen sc1
ds_read_b64_tr_b16 a[176:177], v24 offset:16384
ds_read_b64_tr_b16 a[178:179], v24 offset:18432
v_mfma_f32_16x16x32_bf16 v[36:39], a[20:23], v[176:179], v[36:39]
ds_read_b64_tr_b16 a[180:181], v25 offset:16384
ds_read_b64_tr_b16 a[182:183], v25 offset:18432
v_mfma_f32_16x16x32_bf16 v[40:43], a[24:27], v[176:179], v[40:43]
ds_read_b64_tr_b16 a[184:185], v26 offset:16384
ds_read_b64_tr_b16 a[186:187], v26 offset:18432
v_mfma_f32_16x16x32_bf16 v[44:47], a[28:31], v[176:179], v[44:47]
ds_read_b64_tr_b16 a[188:189], v27 offset:16384
ds_read_b64_tr_b16 a[190:191], v27 offset:18432
s_waitcnt vmcnt(8)
v_mfma_f32_16x16x32_bf16 v[60:63], a[28:31], v[180:183], v[60:63]
buffer_load_dwordx4 v[216:219], v4, s[36:39], s50 offen sc1
ds_read_b64_tr_b16 a[192:193], v24 offset:0
ds_read_b64_tr_b16 a[194:195], v24 offset:2048
v_mfma_f32_16x16x32_bf16 v[56:59], a[24:27], v[180:183], v[56:59]
ds_read_b64_tr_b16 a[196:197], v25 offset:0
ds_read_b64_tr_b16 a[198:199], v25 offset:2048
v_mfma_f32_16x16x32_bf16 v[52:55], a[20:23], v[180:183], v[52:55]
ds_read_b64_tr_b16 a[200:201], v26 offset:0
ds_read_b64_tr_b16 a[202:203], v26 offset:2048
v_mfma_f32_16x16x32_bf16 v[48:51], a[16:19], v[180:183], v[48:51]
ds_read_b64_tr_b16 a[204:205], v27 offset:0
ds_read_b64_tr_b16 a[206:207], v27 offset:2048
s_waitcnt vmcnt(8)
v_mfma_f32_16x16x32_bf16 v[64:67], a[16:19], v[184:187], v[64:67]
buffer_load_dwordx4 v[220:223], v5, s[36:39], s50 offen sc1
s_add_u32 s50, s50, 57344
ds_read_b64_tr_b16 a[208:209], v24 offset:20480
ds_read_b64_tr_b16 a[210:211], v24 offset:22528
v_mfma_f32_16x16x32_bf16 v[68:71], a[20:23], v[184:187], v[68:71]
ds_read_b64_tr_b16 a[212:213], v25 offset:20480
ds_read_b64_tr_b16 a[214:215], v25 offset:22528
v_mfma_f32_16x16x32_bf16 v[72:75], a[24:27], v[184:187], v[72:75]
ds_read_b64_tr_b16 a[216:217], v26 offset:20480
ds_read_b64_tr_b16 a[218:219], v26 offset:22528
v_mfma_f32_16x16x32_bf16 v[76:79], a[28:31], v[184:187], v[76:79]
ds_read_b64_tr_b16 a[220:221], v27 offset:20480
ds_read_b64_tr_b16 a[222:223], v27 offset:22528
s_waitcnt vmcnt(8)
v_mfma_f32_16x16x32_bf16 v[92:95], a[28:31], v[188:191], v[92:95]
buffer_load_dwordx4 v[224:227], v2, s[36:39], s34 offen sc1
v_mfma_f32_16x16x32_bf16 v[88:91], a[24:27], v[188:191], v[88:91]
v_mfma_f32_16x16x32_bf16 v[84:87], a[20:23], v[188:191], v[84:87]
v_mfma_f32_16x16x32_bf16 v[80:83], a[16:19], v[188:191], v[80:83]
s_waitcnt vmcnt(8)
v_mfma_f32_16x16x32_bf16 v[32:35], a[32:35], v[192:195], v[32:35]
buffer_load_dwordx4 v[228:231], v3, s[36:39], s34 offen sc1
v_mfma_f32_16x16x32_bf16 v[36:39], a[36:39], v[192:195], v[36:39]
v_mfma_f32_16x16x32_bf16 v[40:43], a[40:43], v[192:195], v[40:43]
v_mfma_f32_16x16x32_bf16 v[44:47], a[44:47], v[192:195], v[44:47]
s_waitcnt vmcnt(8)
v_mfma_f32_16x16x32_bf16 v[60:63], a[44:47], v[196:199], v[60:63]
buffer_load_dwordx4 v[232:235], v4, s[36:39], s34 offen sc1
s_mov_b64 exec, 1
v_mov_b32 v30, 0
buffer_atomic_add v30, off, s[36:39], s56 sc0 sc1
s_mov_b64 exec, -1
v_mfma_f32_16x16x32_bf16 v[56:59], a[40:43], v[196:199], v[56:59]
v_mfma_f32_16x16x32_bf16 v[52:55], a[36:39], v[196:199], v[52:55]
v_mfma_f32_16x16x32_bf16 v[48:51], a[32:35], v[196:199], v[48:51]
s_waitcnt vmcnt(9)
v_mfma_f32_16x16x32_bf16 v[64:67], a[32:35], v[200:203], v[64:67]
buffer_load_dwordx4 v[236:239], v5, s[36:39], s34 offen sc1
s_add_u32 s34, s34, 57344
v_mfma_f32_16x16x32_bf16 v[68:71], a[36:39], v[200:203], v[68:71]
v_mfma_f32_16x16x32_bf16 v[72:75], a[40:43], v[200:203], v[72:75]
v_mfma_f32_16x16x32_bf16 v[76:79], a[44:47], v[200:203], v[76:79]
s_waitcnt vmcnt(9)
v_mfma_f32_16x16x32_bf16 v[92:95], a[44:47], v[204:207], v[92:95]
buffer_load_dwordx4 v[160:163], v2, s[36:39], s35 offen sc1
v_mfma_f32_16x16x32_bf16 v[88:91], a[40:43], v[204:207], v[88:91]
v_mfma_f32_16x16x32_bf16 v[84:87], a[36:39], v[204:207], v[84:87]
v_mfma_f32_16x16x32_bf16 v[80:83], a[32:35], v[204:207], v[80:83]
s_waitcnt vmcnt(9)
v_mfma_f32_16x16x32_bf16 v[32:35], a[48:51], v[208:211], v[32:35]
buffer_load_dwordx4 v[164:167], v3, s[36:39], s35 offen sc1
v_mfma_f32_16x16x32_bf16 v[36:39], a[52:55], v[208:211], v[36:39]
v_mfma_f32_16x16x32_bf16 v[40:43], a[56:59], v[208:211], v[40:43]
v_mfma_f32_16x16x32_bf16 v[44:47], a[60:63], v[208:211], v[44:47]
s_waitcnt vmcnt(9)
v_mfma_f32_16x16x32_bf16 v[60:63], a[60:63], v[212:215], v[60:63]
buffer_load_dwordx4 v[168:171], v4, s[36:39], s35 offen sc1
v_mfma_f32_16x16x32_bf16 v[56:59], a[56:59], v[212:215], v[56:59]
v_mfma_f32_16x16x32_bf16 v[52:55], a[52:55], v[212:215], v[52:55]
v_mfma_f32_16x16x32_bf16 v[48:51], a[48:51], v[212:215], v[48:51]
s_waitcnt vmcnt(9)
v_mfma_f32_16x16x32_bf16 v[64:67], a[48:51], v[216:219], v[64:67]
buffer_load_dwordx4 v[172:175], v5, s[36:39], s35 offen sc1
s_add_u32 s35, s35, 57344
v_mfma_f32_16x16x32_bf16 v[68:71], a[52:55], v[216:219], v[68:71]
v_mfma_f32_16x16x32_bf16 v[72:75], a[56:59], v[216:219], v[72:75]
v_mfma_f32_16x16x32_bf16 v[76:79], a[60:63], v[216:219], v[76:79]
s_waitcnt vmcnt(9)
v_mfma_f32_16x16x32_bf16 v[92:95], a[60:63], v[220:223], v[92:95]
buffer_load_dwordx4 v[176:179], v2, s[36:39], s3 offen sc1
v_mfma_f32_16x16x32_bf16 v[88:91], a[56:59], v[220:223], v[88:91]
v_mfma_f32_16x16x32_bf16 v[84:87], a[52:55], v[220:223], v[84:87]
v_mfma_f32_16x16x32_bf16 v[80:83], a[48:51], v[220:223], v[80:83]
s_waitcnt vmcnt(9)
s_waitcnt lgkmcnt(14)
v_mfma_f32_16x16x32_bf16 v[32:35], a[64:67], v[224:227], v[32:35]
buffer_load_dwordx4 v[180:183], v3, s[36:39], s3 offen sc1
v_mfma_f32_16x16x32_bf16 v[36:39], a[68:71], v[224:227], v[36:39]
v_mfma_f32_16x16x32_bf16 v[40:43], a[72:75], v[224:227], v[40:43]
v_mfma_f32_16x16x32_bf16 v[44:47], a[76:79], v[224:227], v[44:47]
s_waitcnt vmcnt(9)
v_mfma_f32_16x16x32_bf16 v[60:63], a[76:79], v[228:231], v[60:63]
buffer_load_dwordx4 v[184:187], v4, s[36:39], s3 offen sc1
v_mfma_f32_16x16x32_bf16 v[56:59], a[72:75], v[228:231], v[56:59]
v_mfma_f32_16x16x32_bf16 v[52:55], a[68:71], v[228:231], v[52:55]
v_mfma_f32_16x16x32_bf16 v[48:51], a[64:67], v[228:231], v[48:51]
s_waitcnt vmcnt(9)
v_mfma_f32_16x16x32_bf16 v[64:67], a[64:67], v[232:235], v[64:67]
buffer_load_dwordx4 v[188:191], v5, s[36:39], s3 offen sc1
s_add_u32 s3, s3, 57344
v_mfma_f32_16x16x32_bf16 v[68:71], a[68:71], v[232:235], v[68:71]
v_mfma_f32_16x16x32_bf16 v[72:75], a[72:75], v[232:235], v[72:75]
v_mfma_f32_16x16x32_bf16 v[76:79], a[76:79], v[232:235], v[76:79]
s_waitcnt vmcnt(8)
v_mfma_f32_16x16x32_bf16 v[92:95], a[76:79], v[236:239], v[92:95]
v_readfirstlane_b32 s60, v30
s_cmp_lt_u32 s60, 1792
s_cbranch_scc1 .L_bslow_%=
.L_bok_%=:
buffer_load_dwordx4 v[192:195], v2, s[36:39], s47 offen sc1
v_mfma_f32_16x16x32_bf16 v[88:91], a[72:75], v[236:239], v[88:91]
v_mfma_f32_16x16x32_bf16 v[84:87], a[68:71], v[236:239], v[84:87]
v_mfma_f32_16x16x32_bf16 v[80:83], a[64:67], v[236:239], v[80:83]
s_waitcnt vmcnt(8)
v_mfma_f32_16x16x32_bf16 v[32:35], a[80:83], v[160:163], v[32:35]
buffer_load_dwordx4 v[196:199], v2, s[36:39], s48 offen sc1
v_mfma_f32_16x16x32_bf16 v[36:39], a[84:87], v[160:163], v[36:39]
v_mfma_f32_16x16x32_bf16 v[40:43], a[88:91], v[160:163], v[40:43]
v_mfma_f32_16x16x32_bf16 v[44:47], a[92:95], v[160:163], v[44:47]
s_waitcnt vmcnt(8)
v_mfma_f32_16x16x32_bf16 v[60:63], a[92:95], v[164:167], v[60:63]
buffer_load_dwordx4 v[200:203], v2, s[36:39], s49 offen sc1
v_mfma_f32_16x16x32_bf16 v[56:59], a[88:91], v[164:167], v[56:59]
v_mfma_f32_16x16x32_bf16 v[52:55], a[84:87], v[164:167], v[52:55]
v_mfma_f32_16x16x32_bf16 v[48:51], a[80:83], v[164:167], v[48:51]
s_waitcnt vmcnt(8)
v_mfma_f32_16x16x32_bf16 v[64:67], a[80:83], v[168:171], v[64:67]
buffer_load_dwordx4 v[204:207], v2, s[36:39], s50 offen sc1
v_mfma_f32_16x16x32_bf16 v[68:71], a[84:87], v[168:171], v[68:71]
v_mfma_f32_16x16x32_bf16 v[72:75], a[88:91], v[168:171], v[72:75]
v_mfma_f32_16x16x32_bf16 v[76:79], a[92:95], v[168:171], v[76:79]
s_waitcnt vmcnt(8)
v_mfma_f32_16x16x32_bf16 v[92:95], a[92:95], v[172:175], v[92:95]
buffer_load_dwordx4 v[208:211], v2, s[36:39], s34 offen sc1
v_mfma_f32_16x16x32_bf16 v[88:91], a[88:91], v[172:175], v[88:91]
v_mfma_f32_16x16x32_bf16 v[84:87], a[84:87], v[172:175], v[84:87]
v_mfma_f32_16x16x32_bf16 v[80:83], a[80:83], v[172:175], v[80:83]
s_waitcnt vmcnt(8)
v_mfma_f32_16x16x32_bf16 v[32:35], a[96:99], v[176:179], v[32:35]
buffer_load_dwordx4 v[212:215], v2, s[36:39], s35 offen sc1
v_mfma_f32_16x16x32_bf16 v[36:39], a[100:103], v[176:179], v[36:39]
v_mfma_f32_16x16x32_bf16 v[40:43], a[104:107], v[176:179], v[40:43]
v_mfma_f32_16x16x32_bf16 v[44:47], a[108:111], v[176:179], v[44:47]
s_waitcnt vmcnt(8)
v_mfma_f32_16x16x32_bf16 v[60:63], a[108:111], v[180:183], v[60:63]
buffer_load_dwordx4 v[216:219], v2, s[36:39], s3 offen sc1
v_mfma_f32_16x16x32_bf16 v[56:59], a[104:107], v[180:183], v[56:59]
v_mfma_f32_16x16x32_bf16 v[52:55], a[100:103], v[180:183], v[52:55]
v_mfma_f32_16x16x32_bf16 v[48:51], a[96:99], v[180:183], v[48:51]
s_waitcnt vmcnt(8)
v_mfma_f32_16x16x32_bf16 v[64:67], a[96:99], v[184:187], v[64:67]
buffer_load_dwordx4 v[220:223], v3, s[36:39], s47 offen sc1
v_mfma_f32_16x16x32_bf16 v[68:71], a[100:103], v[184:187], v[68:71]
v_mfma_f32_16x16x32_bf16 v[72:75], a[104:107], v[184:187], v[72:75]
v_mfma_f32_16x16x32_bf16 v[76:79], a[108:111], v[184:187], v[76:79]
s_waitcnt vmcnt(8)
v_mfma_f32_16x16x32_bf16 v[92:95], a[108:111], v[188:191], v[92:95]
buffer_load_dwordx4 v[224:227], v3, s[36:39], s48 offen sc1
v_mfma_f32_16x16x32_bf16 v[88:91], a[104:107], v[188:191], v[88:91]
v_mfma_f32_16x16x32_bf16 v[84:87], a[100:103], v[188:191], v[84:87]
v_mfma_f32_16x16x32_bf16 v[80:83], a[96:99], v[188:191], v[80:83]
s_waitcnt vmcnt(8)
v_mfma_f32_16x16x32_bf16 v[32:35], a[112:115], v[192:195], v[32:35]
buffer_load_dwordx4 v[228:231], v3, s[36:39], s49 offen sc1
v_mfma_f32_16x16x32_bf16 v[36:39], a[116:119], v[192:195], v[36:39]
v_mfma_f32_16x16x32_bf16 v[40:43], a[120:123], v[192:195], v[40:43]
v_mfma_f32_16x16x32_bf16 v[44:47], a[124:127], v[192:195], v[44:47]
s_waitcnt vmcnt(8)
v_mfma_f32_16x16x32_bf16 v[32:35], a[128:131], v[196:199], v[32:35]
buffer_load_dwordx4 v[232:235], v3, s[36:39], s50 offen sc1
v_mfma_f32_16x16x32_bf16 v[36:39], a[132:135], v[196:199], v[36:39]
v_mfma_f32_16x16x32_bf16 v[40:43], a[136:139], v[196:199], v[40:43]
v_mfma_f32_16x16x32_bf16 v[44:47], a[140:143], v[196:199], v[44:47]
s_waitcnt vmcnt(8)
v_mfma_f32_16x16x32_bf16 v[32:35], a[144:147], v[200:203], v[32:35]
buffer_load_dwordx4 v[236:239], v3, s[36:39], s34 offen sc1
v_mfma_f32_16x16x32_bf16 v[36:39], a[148:151], v[200:203], v[36:39]
v_mfma_f32_16x16x32_bf16 v[40:43], a[152:155], v[200:203], v[40:43]
v_mfma_f32_16x16x32_bf16 v[44:47], a[156:159], v[200:203], v[44:47]
s_waitcnt vmcnt(8)
v_mfma_f32_16x16x32_bf16 v[32:35], a[160:163], v[204:207], v[32:35]
buffer_load_dwordx4 v[160:163], v3, s[36:39], s35 offen sc1
v_mfma_f32_16x16x32_bf16 v[36:39], a[164:167], v[204:207], v[36:39]
v_mfma_f32_16x16x32_bf16 v[40:43], a[168:171], v[204:207], v[40:43]
v_mfma_f32_16x16x32_bf16 v[44:47], a[172:175], v[204:207], v[44:47]
s_waitcnt vmcnt(8)
v_mfma_f32_16x16x32_bf16 v[32:35], a[176:179], v[208:211], v[32:35]
buffer_load_dwordx4 v[164:167], v3, s[36:39], s3 offen sc1
v_mfma_f32_16x16x32_bf16 v[36:39], a[180:183], v[208:211], v[36:39]
v_mfma_f32_16x16x32_bf16 v[40:43], a[184:187], v[208:211], v[40:43]
v_mfma_f32_16x16x32_bf16 v[44:47], a[188:191], v[208:211], v[44:47]
s_waitcnt vmcnt(8)
s_waitcnt lgkmcnt(8)
v_mfma_f32_16x16x32_bf16 v[32:35], a[192:195], v[212:215], v[32:35]
buffer_load_dwordx4 v[168:171], v4, s[36:39], s47 offen sc1
v_mfma_f32_16x16x32_bf16 v[36:39], a[196:199], v[212:215], v[36:39]
v_mfma_f32_16x16x32_bf16 v[40:43], a[200:203], v[212:215], v[40:43]
v_mfma_f32_16x16x32_bf16 v[44:47], a[204:207], v[212:215], v[44:47]
s_waitcnt vmcnt(8)
s_waitcnt lgkmcnt(0)
v_mfma_f32_16x16x32_bf16 v[32:35], a[208:211], v[216:219], v[32:35]
buffer_load_dwordx4 v[172:175], v4, s[36:39], s48 offen sc1
v_mfma_f32_16x16x32_bf16 v[36:39], a[212:215], v[216:219], v[36:39]
v_mfma_f32_16x16x32_bf16 v[40:43], a[216:219], v[216:219], v[40:43]
v_mfma_f32_16x16x32_bf16 v[44:47], a[220:223], v[216:219], v[44:47]
s_waitcnt vmcnt(8)
v_mfma_f32_16x16x32_bf16 v[48:51], a[112:115], v[220:223], v[48:51]
buffer_load_dwordx4 v[176:179], v4, s[36:39], s49 offen sc1
v_mfma_f32_16x16x32_bf16 v[52:55], a[116:119], v[220:223], v[52:55]
v_mfma_f32_16x16x32_bf16 v[56:59], a[120:123], v[220:223], v[56:59]
v_mfma_f32_16x16x32_bf16 v[60:63], a[124:127], v[220:223], v[60:63]
s_waitcnt vmcnt(8)
v_mfma_f32_16x16x32_bf16 v[48:51], a[128:131], v[224:227], v[48:51]
buffer_load_dwordx4 v[180:183], v4, s[36:39], s50 offen sc1
ds_write_b128 v10, v[32:35] offset:0
v_mfma_f32_16x16x32_bf16 v[52:55], a[132:135], v[224:227], v[52:55]
v_mfma_f32_16x16x32_bf16 v[56:59], a[136:139], v[224:227], v[56:59]
ds_write_b128 v10, v[36:39] offset:1024
v_mfma_f32_16x16x32_bf16 v[60:63], a[140:143], v[224:227], v[60:63]
s_waitcnt vmcnt(8)
v_mfma_f32_16x16x32_bf16 v[48:51], a[144:147], v[228:231], v[48:51]
buffer_load_dwordx4 v[184:187], v4, s[36:39], s34 offen sc1
ds_write_b128 v10, v[40:43] offset:2048
v_mfma_f32_16x16x32_bf16 v[52:55], a[148:151], v[228:231], v[52:55]
v_mfma_f32_16x16x32_bf16 v[56:59], a[152:155], v[228:231], v[56:59]
ds_write_b128 v10, v[44:47] offset:3072
v_mfma_f32_16x16x32_bf16 v[60:63], a[156:159], v[228:231], v[60:63]
s_waitcnt vmcnt(8)
v_mfma_f32_16x16x32_bf16 v[48:51], a[160:163], v[232:235], v[48:51]
buffer_load_dwordx4 v[188:191], v4, s[36:39], s35 offen sc1
v_mfma_f32_16x16x32_bf16 v[52:55], a[164:167], v[232:235], v[52:55]
v_mfma_f32_16x16x32_bf16 v[56:59], a[168:171], v[232:235], v[56:59]
v_mfma_f32_16x16x32_bf16 v[60:63], a[172:175], v[232:235], v[60:63]
s_waitcnt vmcnt(8)
v_mfma_f32_16x16x32_bf16 v[48:51], a[176:179], v[236:239], v[48:51]
buffer_load_dwordx4 v[192:195], v4, s[36:39], s3 offen sc1
v_mfma_f32_16x16x32_bf16 v[52:55], a[180:183], v[236:239], v[52:55]
v_mfma_f32_16x16x32_bf16 v[56:59], a[184:187], v[236:239], v[56:59]
v_mfma_f32_16x16x32_bf16 v[60:63], a[188:191], v[236:239], v[60:63]
s_waitcnt vmcnt(8)
v_mfma_f32_16x16x32_bf16 v[48:51], a[192:195], v[160:163], v[48:51]
buffer_load_dwordx4 v[196:199], v5, s[36:39], s47 offen sc1
v_mfma_f32_16x16x32_bf16 v[52:55], a[196:199], v[160:163], v[52:55]
v_mfma_f32_16x16x32_bf16 v[56:59], a[200:203], v[160:163], v[56:59]
v_mfma_f32_16x16x32_bf16 v[60:63], a[204:207], v[160:163], v[60:63]
s_waitcnt vmcnt(8)
v_mfma_f32_16x16x32_bf16 v[48:51], a[208:211], v[164:167], v[48:51]
buffer_load_dwordx4 v[200:203], v5, s[36:39], s48 offen sc1
v_mfma_f32_16x16x32_bf16 v[52:55], a[212:215], v[164:167], v[52:55]
v_mfma_f32_16x16x32_bf16 v[56:59], a[216:219], v[164:167], v[56:59]
v_mfma_f32_16x16x32_bf16 v[60:63], a[220:223], v[164:167], v[60:63]
s_waitcnt vmcnt(8)
v_mfma_f32_16x16x32_bf16 v[64:67], a[112:115], v[168:171], v[64:67]
buffer_load_dwordx4 v[204:207], v5, s[36:39], s49 offen sc1
v_mfma_f32_16x16x32_bf16 v[68:71], a[116:119], v[168:171], v[68:71]
v_mfma_f32_16x16x32_bf16 v[72:75], a[120:123], v[168:171], v[72:75]
v_mfma_f32_16x16x32_bf16 v[76:79], a[124:127], v[168:171], v[76:79]
s_waitcnt vmcnt(8)
v_mfma_f32_16x16x32_bf16 v[64:67], a[128:131], v[172:175], v[64:67]
buffer_load_dwordx4 v[208:211], v5, s[36:39], s50 offen sc1
ds_write_b128 v11, v[48:51] offset:4096
v_mfma_f32_16x16x32_bf16 v[68:71], a[132:135], v[172:175], v[68:71]
v_mfma_f32_16x16x32_bf16 v[72:75], a[136:139], v[172:175], v[72:75]
ds_write_b128 v11, v[52:55] offset:5120
v_mfma_f32_16x16x32_bf16 v[76:79], a[140:143], v[172:175], v[76:79]
s_waitcnt vmcnt(8)
v_mfma_f32_16x16x32_bf16 v[64:67], a[144:147], v[176:179], v[64:67]
buffer_load_dwordx4 v[212:215], v5, s[36:39], s34 offen sc1
ds_write_b128 v11, v[56:59] offset:6144
v_mfma_f32_16x16x32_bf16 v[68:71], a[148:151], v[176:179], v[68:71]
v_mfma_f32_16x16x32_bf16 v[72:75], a[152:155], v[176:179], v[72:75]
ds_write_b128 v11, v[60:63] offset:7168
v_mfma_f32_16x16x32_bf16 v[76:79], a[156:159], v[176:179], v[76:79]
s_waitcnt vmcnt(8)
v_mfma_f32_16x16x32_bf16 v[64:67], a[160:163], v[180:183], v[64:67]
buffer_load_dwordx4 v[216:219], v5, s[36:39], s35 offen sc1
v_mfma_f32_16x16x32_bf16 v[68:71], a[164:167], v[180:183], v[68:71]
v_mfma_f32_16x16x32_bf16 v[72:75], a[168:171], v[180:183], v[72:75]
v_mfma_f32_16x16x32_bf16 v[76:79], a[172:175], v[180:183], v[76:79]
s_waitcnt vmcnt(8)
v_mfma_f32_16x16x32_bf16 v[64:67], a[176:179], v[184:187], v[64:67]
buffer_load_dwordx4 v[220:223], v5, s[36:39], s3 offen sc1
v_mfma_f32_16x16x32_bf16 v[68:71], a[180:183], v[184:187], v[68:71]
v_mfma_f32_16x16x32_bf16 v[72:75], a[184:187], v[184:187], v[72:75]
v_mfma_f32_16x16x32_bf16 v[76:79], a[188:191], v[184:187], v[76:79]
s_waitcnt vmcnt(8)
v_mfma_f32_16x16x32_bf16 v[64:67], a[192:195], v[188:191], v[64:67]
v_mfma_f32_16x16x32_bf16 v[68:71], a[196:199], v[188:191], v[68:71]
v_mfma_f32_16x16x32_bf16 v[72:75], a[200:203], v[188:191], v[72:75]
v_mfma_f32_16x16x32_bf16 v[76:79], a[204:207], v[188:191], v[76:79]
s_waitcnt vmcnt(7)
v_mfma_f32_16x16x32_bf16 v[64:67], a[208:211], v[192:195], v[64:67]
v_mfma_f32_16x16x32_bf16 v[68:71], a[212:215], v[192:195], v[68:71]
v_mfma_f32_16x16x32_bf16 v[72:75], a[216:219], v[192:195], v[72:75]
v_mfma_f32_16x16x32_bf16 v[76:79], a[220:223], v[192:195], v[76:79]
s_waitcnt vmcnt(6)
v_mfma_f32_16x16x32_bf16 v[80:83], a[112:115], v[196:199], v[80:83]
v_mfma_f32_16x16x32_bf16 v[84:87], a[116:119], v[196:199], v[84:87]
v_mfma_f32_16x16x32_bf16 v[88:91], a[120:123], v[196:199], v[88:91]
v_mfma_f32_16x16x32_bf16 v[92:95], a[124:127], v[196:199], v[92:95]
s_waitcnt vmcnt(5)
v_mfma_f32_16x16x32_bf16 v[80:83], a[128:131], v[200:203], v[80:83]
ds_write_b128 v12, v[64:67] offset:8192
v_mfma_f32_16x16x32_bf16 v[84:87], a[132:135], v[200:203], v[84:87]
v_mfma_f32_16x16x32_bf16 v[88:91], a[136:139], v[200:203], v[88:91]
ds_write_b128 v12, v[68:71] offset:9216
v_mfma_f32_16x16x32_bf16 v[92:95], a[140:143], v[200:203], v[92:95]
s_waitcnt vmcnt(4)
v_mfma_f32_16x16x32_bf16 v[80:83], a[144:147], v[204:207], v[80:83]
ds_write_b128 v12, v[72:75] offset:10240
v_mfma_f32_16x16x32_bf16 v[84:87], a[148:151], v[204:207], v[84:87]
v_mfma_f32_16x16x32_bf16 v[88:91], a[152:155], v[204:207], v[88:91]
ds_write_b128 v12, v[76:79] offset:11264
v_mfma_f32_16x16x32_bf16 v[92:95], a[156:159], v[204:207], v[92:95]
s_waitcnt vmcnt(3)
v_mfma_f32_16x16x32_bf16 v[80:83], a[160:163], v[208:211], v[80:83]
v_mfma_f32_16x16x32_bf16 v[84:87], a[164:167], v[208:211], v[84:87]
v_mfma_f32_16x16x32_bf16 v[88:91], a[168:171], v[208:211], v[88:91]
v_mfma_f32_16x16x32_bf16 v[92:95], a[172:175], v[208:211], v[92:95]
s_waitcnt vmcnt(2)
v_mfma_f32_16x16x32_bf16 v[80:83], a[176:179], v[212:215], v[80:83]
s_waitcnt lgkmcnt(0)
s_barrier
ds_read_b128 v[96:99], v13 offset:0
ds_read_b128 v[112:115], v13 offset:4096
v_mfma_f32_16x16x32_bf16 v[84:87], a[180:183], v[212:215], v[84:87]
ds_read_b128 v[128:131], v13 offset:8192
ds_read_b128 v[100:103], v13 offset:1024
v_mfma_f32_16x16x32_bf16 v[88:91], a[184:187], v[212:215], v[88:91]
ds_read_b128 v[116:119], v13 offset:5120
ds_read_b128 v[132:135], v13 offset:9216
v_mfma_f32_16x16x32_bf16 v[92:95], a[188:191], v[212:215], v[92:95]
ds_read_b128 v[104:107], v13 offset:2048
ds_read_b128 v[120:123], v13 offset:6144
s_waitcnt vmcnt(1)
v_mfma_f32_16x16x32_bf16 v[80:83], a[192:195], v[216:219], v[80:83]
ds_read_b128 v[136:139], v13 offset:10240
ds_read_b128 v[108:111], v13 offset:3072
v_mfma_f32_16x16x32_bf16 v[84:87], a[196:199], v[216:219], v[84:87]
ds_read_b128 v[124:127], v13 offset:7168
ds_read_b128 v[140:143], v13 offset:11264
v_mfma_f32_16x16x32_bf16 v[88:91], a[200:203], v[216:219], v[88:91]
v_mfma_f32_16x16x32_bf16 v[92:95], a[204:207], v[216:219], v[92:95]
s_waitcnt vmcnt(0)
v_mfma_f32_16x16x32_bf16 v[80:83], a[208:211], v[220:223], v[80:83]
v_mfma_f32_16x16x32_bf16 v[84:87], a[212:215], v[220:223], v[84:87]
v_mfma_f32_16x16x32_bf16 v[88:91], a[216:219], v[220:223], v[88:91]
v_mfma_f32_16x16x32_bf16 v[92:95], a[220:223], v[220:223], v[92:95]
s_waitcnt lgkmcnt(9)
v_add_f32 v96, v96, v112
v_add_f32 v97, v97, v113
v_add_f32 v98, v98, v114
v_add_f32 v99, v99, v115
v_add_f32 v96, v96, v128
v_add_f32 v97, v97, v129
v_add_f32 v98, v98, v130
v_add_f32 v99, v99, v131
s_waitcnt lgkmcnt(6)
v_add_f32 v100, v100, v116
v_add_f32 v101, v101, v117
v_add_f32 v102, v102, v118
v_add_f32 v103, v103, v119
v_add_f32 v100, v100, v132
v_add_f32 v101, v101, v133
v_add_f32 v102, v102, v134
v_add_f32 v103, v103, v135
s_waitcnt lgkmcnt(3)
v_add_f32 v104, v104, v120
v_add_f32 v105, v105, v121
v_add_f32 v106, v106, v122
v_add_f32 v107, v107, v123
v_add_f32 v104, v104, v136
v_add_f32 v105, v105, v137
v_add_f32 v106, v106, v138
v_add_f32 v107, v107, v139
s_waitcnt lgkmcnt(0)
v_add_f32 v108, v108, v124
v_add_f32 v109, v109, v125
v_add_f32 v110, v110, v126
v_add_f32 v111, v111, v127
v_add_f32 v108, v108, v140
v_add_f32 v109, v109, v141
v_add_f32 v110, v110, v142
v_add_f32 v111, v111, v143
v_add_f32 v96, v96, v80
v_add_f32 v97, v97, v81
v_add_f32 v98, v98, v82
v_add_f32 v99, v99, v83
v_add_f32 v100, v100, v84
v_add_f32 v101, v101, v85
v_add_f32 v102, v102, v86
v_add_f32 v103, v103, v87
v_cvt_pk_bf16_f32 v112, v96, v97
v_cvt_pk_bf16_f32 v113, v98, v99
v_cvt_pk_bf16_f32 v114, v100, v101
v_cvt_pk_bf16_f32 v115, v102, v103
s_nop 1
v_permlane16_swap_b32 v112, v114
v_permlane16_swap_b32 v113, v115
buffer_store_dwordx4 v[112:115], v14, s[28:31], s59 offen offset:0
v_add_f32 v104, v104, v88
v_add_f32 v105, v105, v89
v_add_f32 v106, v106, v90
v_add_f32 v107, v107, v91
v_add_f32 v108, v108, v92
v_add_f32 v109, v109, v93
v_add_f32 v110, v110, v94
v_add_f32 v111, v111, v95
v_cvt_pk_bf16_f32 v116, v104, v105
v_cvt_pk_bf16_f32 v117, v106, v107
v_cvt_pk_bf16_f32 v118, v108, v109
v_cvt_pk_bf16_f32 v119, v110, v111
s_nop 1
v_permlane16_swap_b32 v116, v118
v_permlane16_swap_b32 v117, v119
buffer_store_dwordx4 v[116:119], v14, s[28:31], s59 offen offset:64
s_endpgm
.L_ackslow_p_%=:
s_mov_b32 s61, 65536
.L_ackspin_p_%=:
s_sleep 4
buffer_load_dword v9, v6, s[36:39], s57 offen sc1
s_waitcnt vmcnt(0)
v_readfirstlane_b32 s60, v9
v_mov_b32 v9, 1
s_cmp_lg_u32 s60, 0
s_cbranch_scc1 .L_ackok_p_%=
s_sub_u32 s61, s61, 1
s_cbranch_scc0 .L_ackspin_p_%=
s_branch .L_ackok_p_%=
.L_ackslow_h_%=:
s_mov_b32 s61, 65536
.L_ackspin_h_%=:
s_sleep 4
buffer_load_dword v9, v6, s[36:39], s57 offen sc1
s_waitcnt vmcnt(0)
v_readfirstlane_b32 s60, v9
v_mov_b32 v9, 1
s_cmp_lg_u32 s60, 0
s_cbranch_scc1 .L_ackok_h_%=
s_sub_u32 s61, s61, 1
s_cbranch_scc0 .L_ackspin_h_%=
s_branch .L_ackok_h_%=
.L_bslow_%=:
s_mov_b32 s61, 65536
s_mov_b64 exec, 1
.L_bspin_%=:
v_mov_b32 v30, 0
buffer_atomic_add v30, off, s[36:39], s56 sc0 sc1
s_waitcnt vmcnt(0)
v_readfirstlane_b32 s60, v30
s_cmp_ge_u32 s60, 1792
s_cbranch_scc1 .L_bdone_%=
s_sub_u32 s61, s61, 1
s_cbranch_scc0 .L_bspin_%=
.L_bdone_%=:
s_mov_b64 exec, -1
s_branch .L_bok_%=
.L_end_%=:
)ASM" : "+s"(ka), "+s"(bid), "+v"(tid), "+s"(lb) : : "memory","vcc","scc","v1","v2","v3","v4","v5","v6","v7","v8","v9","v10","v11","v12","v13","v14","v15","v16","v17","v18","v19","v20","v21","v22","v23","v24","v25","v26","v27","v28","v29","v30","v31","v32","v33","v34","v35","v36","v37","v38","v39","v40","v41","v42","v43","v44","v45","v46","v47","v48","v49","v50","v51","v52","v53","v54","v55","v56","v57","v58","v59","v60","v61","v62","v63","v64","v65","v66","v67","v68","v69","v70","v71","v72","v73","v74","v75","v76","v77","v78","v79","v80","v81","v82","v83","v84","v85","v86","v87","v88","v89","v90","v91","v92","v93","v94","v95","v96","v97","v98","v99","v100","v101","v102","v103","v104","v105","v106","v107","v108","v109","v110","v111","v112","v113","v114","v115","v116","v117","v118","v119","v120","v121","v122","v123","v124","v125","v126","v127","v128","v129","v130","v131","v132","v133","v134","v135","v136","v137","v138","v139","v140","v141","v142","v143","v144","v145","v146","v147","v148","v149","v150","v151","v152","v153","v154","v155","v156","v157","v158","v159","v160","v161","v162","v163","v164","v165","v166","v167","v168","v169","v170","v171","v172","v173","v174","v175","v176","v177","v178","v179","v180","v181","v182","v183","v184","v185","v186","v187","v188","v189","v190","v191","v192","v193","v194","v195","v196","v197","v198","v199","v200","v201","v202","v203","v204","v205","v206","v207","v208","v209","v210","v211","v212","v213","v214","v215","v216","v217","v218","v219","v220","v221","v222","v223","v224","v225","v226","v227","v228","v229","v230","v231","v232","v233","v234","v235","v236","v237","v238","v239","v240","v241","v242","v243","v244","v245","v246","v247","v248","v249","v250","v251","v252","v253","v254","v255","a0","a1","a2","a3","a4","a5","a6","a7","a8","a9","a10","a11","a12","a13","a14","a15","a16","a17","a18","a19","a20","a21","a22","a23","a24","a25","a26","a27","a28","a29","a30","a31","a32","a33","a34","a35","a36","a37","a38","a39","a40","a41","a42","a43","a44","a45","a46","a47","a48","a49","a50","a51","a52","a53","a54","a55","a56","a57","a58","a59","a60","a61","a62","a63","a64","a65","a66","a67","a68","a69","a70","a71","a72","a73","a74","a75","a76","a77","a78","a79","a80","a81","a82","a83","a84","a85","a86","a87","a88","a89","a90","a91","a92","a93","a94","a95","a96","a97","a98","a99","a100","a101","a102","a103","a104","a105","a106","a107","a108","a109","a110","a111","a112","a113","a114","a115","a116","a117","a118","a119","a120","a121","a122","a123","a124","a125","a126","a127","a128","a129","a130","a131","a132","a133","a134","a135","a136","a137","a138","a139","a140","a141","a142","a143","a144","a145","a146","a147","a148","a149","a150","a151","a152","a153","a154","a155","a156","a157","a158","a159","a160","a161","a162","a163","a164","a165","a166","a167","a168","a169","a170","a171","a172","a173","a174","a175","a176","a177","a178","a179","a180","a181","a182","a183","a184","a185","a186","a187","a188","a189","a190","a191","a192","a193","a194","a195","a196","a197","a198","a199","a200","a201","a202","a203","a204","a205","a206","a207","a208","a209","a210","a211","a212","a213","a214","a215","a216","a217","a218","a219","a220","a221","a222","a223","a224","a225","a226","a227","a228","a229","a230","a231","a232","a233","a234","a235","a236","a237","a238","a239","a240","a241","a242","a243","a244","a245","a246","a247","a248","a249","a250","a251","a252","a253","a254","a255","s4","s5","s6","s7","s8","s9","s10","s11","s12","s13","s14","s15","s16","s17","s18","s19","s20","s21","s22","s23","s24","s25","s26","s27","s28","s29","s30","s31","s34","s35","s36","s37","s38","s39","s40","s41","s42","s43","s44","s45","s46","s47","s48","s49","s50","s51","s52","s53","s54","s55","s56","s57","s58","s59","s60","s61","s62","s63","s64","s65","s66","s67","s68","s69","s70","s71","s72","s73","s74","s75","s76","s77","s78","s79","s80","s81","s82","s83","s84","s85","s86","s87","s88","s89","s90","s91","s92","s93","s94","s95","s96","s97","s98","s99");
}
extern "C" void kernel_launch(void* const* d_in, const int* in_sizes, int n_in, void* d_out, int out_size, void* d_ws, size_t ws_size, hipStream_t stream){
  k_glu<<<256,256,0,stream>>>(d_in[0],d_in[1],d_in[2],d_in[3],d_out,d_ws);
}
```
